```python
import torch

def _softmax(x, axis=-1):
    return torch.softmax(x, dim=axis)
def _gelu(x):
    return torch.nn.functional.gelu(x, approximate="tanh")
def _silu(x):
    return torch.nn.functional.silu(x)
def _sigmoid(x):
    return torch.sigmoid(x)
def _layernorm(x, axis=-1, eps=1e-5):
    m = x.mean(dim=axis, keepdim=True)
    v = x.var(dim=axis, keepdim=True, unbiased=False)
    return (x - m) / torch.sqrt(v + eps)
def _scatter_add(tgt, idx, src, axis=0):
    return tgt.clone().index_add_(axis, idx, src)
def _segment_sum(src, seg, n):
    out = torch.zeros((n,) + src.shape[1:], dtype=src.dtype)
    return out.index_add_(0, seg, src)
def _pad(x, pw):
    flat = tuple(v for pair in reversed(pw) for v in pair)
    return torch.nn.functional.pad(x, flat)

kernel_name = 'gated_mix__ssm_conv_h3__sliding_block__prompt'

def setup_inputs():

    _w = (lambda a: (lambda t: t.to(torch.bfloat16) if t.is_floating_point() else t)(torch.as_tensor(a)))
    def _A_setup():
        B, S, D = 1, 2048, 512
        ks = 4
        return {
            "q": _w((lambda *s: torch.randn(*s, dtype=torch.bfloat16) * 0.05)(B, S, D)),
            "k": _w((lambda *s: torch.randn(*s, dtype=torch.bfloat16) * 0.8)(B, S, D)),
            "v": _w((lambda *s: torch.randn(*s, dtype=torch.bfloat16) * 0.8)(B, S, D)),
            "shift_k": _w((lambda *s: torch.randn(*s, dtype=torch.bfloat16) * 0.8)(D, ks) * 0.5),
            "ssm_k": _w((lambda *s: torch.randn(*s, dtype=torch.bfloat16) * 1.6)(D, S) * (1.0 / S ** 0.5)),
            "ssm_D": _w((lambda *s: torch.randn(*s, dtype=torch.bfloat16) * 0.8)(D)),
        }
    _A_in = _A_setup()

    def _B_setup():
        B, S, D = 1, 2048, 512
        return {
            "x": _w((lambda *s: torch.randn(*s, dtype=torch.bfloat16) * 0.05)(B, S, D)),
            "wqkv": _w((lambda *s: torch.randn(*s, dtype=torch.bfloat16) * 0.8)(D, 3 * D)),
            "wo": _w((lambda *s: torch.randn(*s, dtype=torch.bfloat16) * 0.05)(D, D)),
        }
    _B_in = _B_setup()

    return {
        "A_q": _A_in["q"],
        "A_k": _A_in["k"],
        "A_v": _A_in["v"],
        "A_shift_k": _A_in["shift_k"],
        "A_ssm_k": _A_in["ssm_k"],
        "A_ssm_D": _A_in["ssm_D"],
        "B_x": _B_in["x"],
        "B_wqkv": _B_in["wqkv"],
        "B_wo": _B_in["wo"],
        "gate_w": _w((lambda *s: torch.randn(*s, dtype=torch.bfloat16) * 0.05)(512) * 16.0),
    }

def reference(A_q, A_k, A_v, A_shift_k, A_ssm_k, A_ssm_D, B_x, B_wqkv, B_wo, gate_w):
    def _A_ref(q, k, v, shift_k, ssm_k, ssm_D):
        B, S, D = q.shape
        ks = shift_k.shape[-1]
        k_pad = _pad(k, ((0, 0), (ks - 1, 0), (0, 0)))
        k_shift = torch.zeros_like(k)
        for t in range(ks):
            k_shift = k_shift + k_pad[:, t:t + S, :] * shift_k[:, ks - 1 - t][None, None, :]
        kv = k_shift * v
        L = 2 * S
        kv_pad = _pad(kv, ((0, 0), (0, S), (0, 0)))
        kf_pad = _pad(ssm_k, ((0, 0), (0, S)))
        kv_f = (lambda x, axes=(-2,-1): torch.fft.rfft2(x.float(), dim=axes))(kv_pad[:, None, :, :], axes=(1, 2))
        kf_f = (lambda x, axes=(-2,-1): torch.fft.rfft2(x.float(), dim=axes))(kf_pad[:, None, :], axes=(1, 2))
        kf_f = (lambda x, *d: x.permute(*d))(kf_f, 1, 2, 0)
        y_f = kv_f * kf_f[None, :, :, :]
        y = (lambda x, s, axes=(-2,-1): torch.fft.irfft2(x, s=s, dim=axes).to(torch.bfloat16))(y_f, (1, L), axes=(1, 2))[:, 0, :S, :]
        y = y + kv * ssm_D[None, None, :]
        return q * y

    def _B_ref(x, wqkv, wo):
        B, S, D = x.shape
        H = 32; Dh = D // H; W = 256
        qkv = (lambda x, *d: x.permute(*d))((x @ wqkv).reshape(B, S, 3, H, Dh), 2, 0, 3, 1, 4)
        q, k, v = qkv[0], qkv[1], qkv[2]
        scores = torch.einsum("bhqd,bhkd->bhqk", q, k) * (Dh ** -0.5)
        idx = torch.arange(S)
        mask = (idx[:, None] - idx[None, :] >= 0) & (idx[:, None] - idx[None, :] < W)
        scores = torch.where(mask[None, None], scores, -1e9)
        p = _softmax(scores, axis=-1)
        o = torch.einsum("bhqk,bhkd->bhqd", p, v)
        return (lambda x, *d: x.permute(*d))(o, 0, 2, 1, 3).reshape(B, S, D) @ wo

    x = A_q
    g = _sigmoid((x * gate_w).sum(-1, keepdims=True))
    a = _A_ref(x, A_k, A_v, A_shift_k, A_ssm_k, A_ssm_D)
    b = _B_ref(x, B_wqkv, B_wo)
    return g * a + (1 - g) * b
```

```cpp
#include <hip/hip_runtime.h>
#include <hip/hip_bf16.h>
#include <stdint.h>

#define SEQ 2048
#define DM 512
#define NH 32
#define DH 16

typedef __attribute__((ext_vector_type(8))) short bf16x8;
typedef __attribute__((ext_vector_type(4))) short s16x4;
typedef __attribute__((ext_vector_type(16))) float f32x16;
typedef __attribute__((ext_vector_type(4))) float f32x4;
typedef __attribute__((ext_vector_type(4))) unsigned u32x4;
typedef __attribute__((ext_vector_type(2))) unsigned u32x2;
typedef __attribute__((ext_vector_type(2))) __bf16 bf16x2_t;
typedef unsigned short u16;

#define DI __device__ __forceinline__
#define MFMA(a, b, c) __builtin_amdgcn_mfma_f32_32x32x16_bf16((a), (b), (c), 0, 0, 0)
#define LDS_AS __attribute__((address_space(3)))
#define GLB_AS __attribute__((address_space(1)))

DI float bf2f(unsigned v16) { return __uint_as_float(v16 << 16); }
DI float bflo(unsigned w) { return __uint_as_float(w << 16); }
DI float bfhi(unsigned w) { return __uint_as_float(w & 0xffff0000u); }
DI unsigned pk2(float lo, float hi) {
  bf16x2_t v; v[0] = (__bf16)lo; v[1] = (__bf16)hi;
  return __builtin_bit_cast(unsigned, v);
}
DI float rbf(float x) {
  bf16x2_t v; v[0] = (__bf16)x; v[1] = (__bf16)0.f;
  return __uint_as_float(__builtin_bit_cast(unsigned, v) << 16);
}

#define WS_Q   (0u)
#define WS_K   (2u << 20)
#define WS_VT  (4u << 20)
#define WS_KVT (6u << 20)
#define WS_QT  (8u << 20)
#define WS_AT  (10u << 20)
#define WS_O   (12u << 20)
#define WS_WOT (14u << 20)
#define WS_G   (15u << 20)

template <int NV>
DI void gemm_body(const u16* __restrict__ X, const char* lds, int m0, int n0, int lane,
                  u16* __restrict__ Qh, u16* __restrict__ Kh, u16* __restrict__ Vt) {
  const int r = lane & 31, hh = lane >> 5;
  const u16* xp = X + (size_t)(m0 + r) * DM + 8 * hh;
  f32x16 acc[3];
#pragma unroll
  for (int j = 0; j < 3; ++j)
#pragma unroll
    for (int i = 0; i < 16; ++i) acc[j][i] = 0.f;
  bf16x8 af[32];
#pragma unroll
  for (int ks = 0; ks < 32; ++ks) af[ks] = *(const bf16x8*)(xp + 16 * ks);

  __builtin_amdgcn_s_waitcnt(0);
  __syncthreads();

  const int g = (lane >> 4) & 1, q = (lane & 15) >> 2, p = lane & 3;
  const unsigned lbase = (unsigned)(size_t)lds + (8 * hh + q) * 192 + (16 * g + 4 * p) * 2;
#pragma unroll
  for (int ks = 0; ks < 32; ++ks) {
#pragma unroll
    for (int j = 0; j < 3; ++j) {
      unsigned a0 = lbase + ks * 16 * 192 + j * 64;
      s16x4 lo = __builtin_amdgcn_ds_read_tr16_b64_v4i16((LDS_AS s16x4*)(a0));
      s16x4 hi = __builtin_amdgcn_ds_read_tr16_b64_v4i16((LDS_AS s16x4*)(a0 + 4 * 192));
      bf16x8 bfr = __builtin_shufflevector(lo, hi, 0, 1, 2, 3, 4, 5, 6, 7);
      if (j >= 3 - NV) acc[j] = MFMA(af[ks], bfr, acc[j]);
      else             acc[j] = MFMA(bfr, af[ks], acc[j]);
    }
  }
#pragma unroll
  for (int j = 0; j < 3; ++j) {
    const int nc = n0 + 32 * j;
    if (j >= 3 - NV) {
      const int nv = nc - 1024 + r;
      const int head = nv >> 4, d = nv & 15;
      u16* dst = Vt + ((size_t)((head * 64 + (m0 >> 5)) * 16 + d)) * 32 + 8 * hh;
      u32x4 w0, w1;
      w0[0] = pk2(acc[j][0], acc[j][1]);  w0[1] = pk2(acc[j][2], acc[j][3]);
      w0[2] = pk2(acc[j][4], acc[j][5]);  w0[3] = pk2(acc[j][6], acc[j][7]);
      w1[0] = pk2(acc[j][8], acc[j][9]);  w1[1] = pk2(acc[j][10], acc[j][11]);
      w1[2] = pk2(acc[j][12], acc[j][13]); w1[3] = pk2(acc[j][14], acc[j][15]);
      *(u32x4*)(dst) = w0;
      *(u32x4*)(dst + 16) = w1;
    } else {
      u16* base = (nc < 512) ? Qh : Kh;
      const int head0 = (nc & 511) >> 4;
#pragma unroll
      for (int gq = 0; gq < 4; ++gq) {
        const int head = head0 + (gq >> 1);
        u16* dst = base + ((size_t)head * SEQ + m0 + r) * DH + 8 * (gq & 1) + 4 * hh;
        u32x2 w;
        w[0] = pk2(acc[j][4 * gq], acc[j][4 * gq + 1]);
        w[1] = pk2(acc[j][4 * gq + 2], acc[j][4 * gq + 3]);
        *(u32x2*)dst = w;
      }
    }
  }
}

__global__ __launch_bounds__(256, 1) void k1_gemm_prep(
    const u16* __restrict__ Aq, const u16* __restrict__ Ak, const u16* __restrict__ Av,
    const u16* __restrict__ shiftk, const u16* __restrict__ wqkv, const u16* __restrict__ wo,
    const u16* __restrict__ gatew, char* __restrict__ ws) {
  __shared__ __attribute__((aligned(16))) char lds[512 * 192];
  const int tid = threadIdx.x, lane = tid & 63, w = tid >> 6;
  const int b = blockIdx.x;
  const int xcd = b & 7, idx = b >> 3;
  const int mt = (xcd >> 1) * 4 + (idx >> 3);
  const int nt = (xcd & 1) * 8 + (idx & 7);
  const int n0 = nt * 96;

#pragma unroll
  for (int i = 0; i < 24; ++i) {
    const int ci = (i * 4 + w) * 64 + lane;
    const int row = ci / 12, c16 = ci - row * 12;
    const u16* src = wqkv + (size_t)row * 1536 + n0 + c16 * 8;
    __builtin_amdgcn_global_load_lds((GLB_AS const void*)src,
                                     (LDS_AS void*)(lds + (i * 4 + w) * 1024), 16, 0, 0);
  }

  {
    const int st = b >> 3, dt = b & 7;
    const int cg = tid & 15, sg = tid >> 4;
    const int d0 = dt * 64 + cg * 4;
    const int s0 = st * 64 + sg * 4;
    u32x2 kk[7], vv[4], qq[4];
#pragma unroll
    for (int i = 0; i < 7; ++i) {
      const int s = s0 - 3 + i;
      if (s >= 0) kk[i] = *(const u32x2*)(Ak + (size_t)s * DM + d0);
      else { kk[i][0] = 0; kk[i][1] = 0; }
    }
#pragma unroll
    for (int i = 0; i < 4; ++i) {
      vv[i] = *(const u32x2*)(Av + (size_t)(s0 + i) * DM + d0);
      qq[i] = *(const u32x2*)(Aq + (size_t)(s0 + i) * DM + d0);
    }
    u16* kvT = (u16*)(ws + WS_KVT);
    u16* qT = (u16*)(ws + WS_QT);
#pragma unroll
    for (int c = 0; c < 4; ++c) {
      const u32x2 skw = *(const u32x2*)(shiftk + (size_t)(d0 + c) * 4);
      const float sk0 = bflo(skw[0]), sk1 = bfhi(skw[0]), sk2 = bflo(skw[1]), sk3 = bfhi(skw[1]);
      float kf[7];
#pragma unroll
      for (int i = 0; i < 7; ++i) kf[i] = (c & 1) ? bfhi(kk[i][c >> 1]) : bflo(kk[i][c >> 1]);
      float kvv[4], qv[4];
#pragma unroll
      for (int i = 0; i < 4; ++i) {
        float acc = rbf(kf[i] * sk3);
        acc = rbf(acc + rbf(kf[i + 1] * sk2));
        acc = rbf(acc + rbf(kf[i + 2] * sk1));
        acc = rbf(acc + rbf(kf[i + 3] * sk0));
        const float vf = (c & 1) ? bfhi(vv[i][c >> 1]) : bflo(vv[i][c >> 1]);
        kvv[i] = acc * vf;
        qv[i] = (c & 1) ? bfhi(qq[i][c >> 1]) : bflo(qq[i][c >> 1]);
      }
      u32x2 o1, o2;
      o1[0] = pk2(kvv[0], kvv[1]); o1[1] = pk2(kvv[2], kvv[3]);
      o2[0] = pk2(qv[0], qv[1]);   o2[1] = pk2(qv[2], qv[3]);
      *(u32x2*)(kvT + (size_t)(d0 + c) * SEQ + s0) = o1;
      *(u32x2*)(qT + (size_t)(d0 + c) * SEQ + s0) = o2;
    }
    if (w == 0) {
      const int kt = b >> 3, ntile = b & 7;
      const int cgn = lane & 15, kg = lane >> 4;
      const int k0 = kt * 16 + kg * 4, nn0 = ntile * 64 + cgn * 4;
      u32x2 wv[4];
#pragma unroll
      for (int i = 0; i < 4; ++i) wv[i] = *(const u32x2*)(wo + (size_t)(k0 + i) * DM + nn0);
      u16* woT = (u16*)(ws + WS_WOT);
#pragma unroll
      for (int c = 0; c < 4; ++c) {
        u32x2 o;
        unsigned e[4];
#pragma unroll
        for (int i = 0; i < 4; ++i) e[i] = (c & 1) ? (wv[i][c >> 1] >> 16) : (wv[i][c >> 1] & 0xffffu);
        o[0] = e[0] | (e[1] << 16); o[1] = e[2] | (e[3] << 16);
        *(u32x2*)(woT + (size_t)(nn0 + c) * DM + k0) = o;
      }
    }
    {
      const u32x4 gw = *(const u32x4*)(gatew + lane * 8);
      float* gout = (float*)(ws + WS_G);
#pragma unroll
      for (int i = 0; i < 2; ++i) {
        const int s = b * 8 + w * 2 + i;
        const u32x4 xv = *(const u32x4*)(Aq + (size_t)s * DM + lane * 8);
        float sum = 0.f;
#pragma unroll
        for (int e = 0; e < 4; ++e) {
          sum += rbf(bflo(xv[e]) * bflo(gw[e]));
          sum += rbf(bfhi(xv[e]) * bfhi(gw[e]));
        }
#pragma unroll
        for (int o = 32; o > 0; o >>= 1) sum += __shfl_xor(sum, o);
        const float z = rbf(sum);
        const float gv = rbf(1.f / (1.f + __expf(-z)));
        if (lane == 0) gout[s] = gv;
      }
    }
  }

  u16* Qh = (u16*)(ws + WS_Q);
  u16* Kh = (u16*)(ws + WS_K);
  u16* Vt = (u16*)(ws + WS_VT);
  const int m0 = mt * 128 + w * 32;
  if (nt <= 9) gemm_body<0>(Aq, lds, m0, n0, lane, Qh, Kh, Vt);
  else if (nt == 10) gemm_body<1>(Aq, lds, m0, n0, lane, Qh, Kh, Vt);
  else gemm_body<3>(Aq, lds, m0, n0, lane, Qh, Kh, Vt);
}

#define CSTR 4608
#define XR_OFF (8 * CSTR)
#define XR_STR 80
#define K2_LDS (XR_OFF + 95 * XR_STR)

DI void conv_body(int d, char* lds, const u16* __restrict__ ssmk, const u16* __restrict__ ssmD,
                  char* __restrict__ ws) {
  const int tid = threadIdx.x, lane = tid & 63, w = tid >> 6;
  const int r = lane & 31, hh = lane >> 5;
  const u16* hrow = ssmk + (size_t)d * SEQ;
  const u16* xrow = (const u16*)(ws + WS_KVT) + (size_t)d * SEQ;
  {
    const u32x4 z4 = {0u, 0u, 0u, 0u};
    for (int t = tid; t < 260; t += 256) {
      u32x4 h0 = (t >= 4 && t < 260) ? *(const u32x4*)(hrow + 8 * (t - 4)) : z4;
      u32x4 h1 = (t >= 3 && t < 259) ? *(const u32x4*)(hrow + 8 * (t - 3)) : z4;
      unsigned wv[8] = {h0[0], h0[1], h0[2], h0[3], h1[0], h1[1], h1[2], h1[3]};
      unsigned od[7];
#pragma unroll
      for (int i = 0; i < 7; ++i) od[i] = __builtin_amdgcn_alignbyte(wv[i + 1], wv[i], 2);
#pragma unroll
      for (int c = 0; c < 8; ++c) {
        const int e = c >> 1;
        u32x4 v;
        if (c & 1) { v[0] = od[e]; v[1] = od[e + 1]; v[2] = od[e + 2]; v[3] = od[e + 3]; }
        else       { v[0] = wv[e]; v[1] = wv[e + 1]; v[2] = wv[e + 2]; v[3] = wv[e + 3]; }
        const unsigned off = (0x5D9159D0u >> (4 * c)) & 15u;
        *(u32x4*)(lds + c * CSTR + off * 16 + t * 16) = v;
      }
    }
    {
      const u32x4 xv = *(const u32x4*)(xrow + 8 * tid);
      u32x4 rv;
#pragma unroll
      for (int i = 0; i < 4; ++i) rv[i] = __builtin_amdgcn_alignbit(xv[3 - i], xv[3 - i], 16);
      *(u32x4*)(lds + XR_OFF + ((tid >> 2) + 31) * XR_STR + (3 - (tid & 3)) * 16) = rv;
      if (tid < 124) *(u32x4*)(lds + XR_OFF + (tid >> 2) * XR_STR + (tid & 3) * 16) = z4;
    }
  }
  __syncthreads();

  f32x16 acc0, acc1a, acc1b;
#pragma unroll
  for (int i = 0; i < 16; ++i) { acc0[i] = 0.f; acc1a[i] = 0.f; acc1b[i] = 0.f; }
  {
    const int c = (1 + r) & 7, u = (1 + r) >> 3;
    const unsigned off = (0x5D9159D0u >> (4 * c)) & 15u;
    const char* abase = lds + c * CSTR + off * 16 + (16 + 8 * hh + 8 * u) * 2 + w * 8 * 64;
    const char* bbase = lds + XR_OFF + (r + 31 - w * 8) * XR_STR + 16 * hh;
#pragma unroll
    for (int gi = 0; gi < 8; ++gi) {
#pragma unroll
      for (int ks = 0; ks < 2; ++ks) {
        const bf16x8 A0 = *(const bf16x8*)(abase + gi * 64 - ks * 32);
        const bf16x8 A1 = *(const bf16x8*)(abase + gi * 64 - ks * 32 + 32 * 64);
        const bf16x8 B0 = *(const bf16x8*)(bbase - gi * XR_STR + (1 - ks) * 32);
        const bf16x8 B1 = *(const bf16x8*)(bbase + (32 - gi) * XR_STR + (1 - ks) * 32);
        acc0 = MFMA(A0, B0, acc0);
        acc1a = MFMA(A1, B0, acc1a);
        acc1b = MFMA(A0, B1, acc1b);
      }
    }
  }
  __syncthreads();
  {
    float* red = (float*)lds;
#pragma unroll
    for (int dst = 0; dst < 4; ++dst)
#pragma unroll
      for (int hf = 0; hf < 2; ++hf) {
        f32x4 v;
#pragma unroll
        for (int i = 0; i < 4; ++i) {
          const int rg = 8 * (dst & 1) + 4 * hf + i;
          v[i] = (dst >> 1) ? (acc1a[rg] + acc1b[rg]) : acc0[rg];
        }
        *(f32x4*)(red + ((((w * 4 + dst) * 2 + hf) * 64) + lane) * 4) = v;
      }
  }
  __syncthreads();
  {
    const float* red = (const float*)lds;
    const float Dv = bf2f(ssmD[d]);
    const u16* qT = (const u16*)(ws + WS_QT) + (size_t)d * SEQ;
    u16* aT = (u16*)(ws + WS_AT) + (size_t)d * SEQ;
    const int ablk = 32 * (w >> 1) + r;
#pragma unroll
    for (int hf = 0; hf < 2; ++hf) {
      f32x4 y = {0.f, 0.f, 0.f, 0.f};
#pragma unroll
      for (int src = 0; src < 4; ++src) {
        const f32x4 v = *(const f32x4*)(red + ((((src * 4 + w) * 2 + hf) * 64) + lane) * 4);
        y += v;
      }
      const int b0 = 16 * (w & 1) + 8 * hf + 4 * hh;
      const int s = 32 * ablk + b0;
      const u32x2 qv = *(const u32x2*)(qT + s);
      const u32x2 xv = *(const u32x2*)(lds + XR_OFF + (ablk + 31) * XR_STR + (28 - b0) * 2);
      float kv[4] = {bfhi(xv[1]), bflo(xv[1]), bfhi(xv[0]), bflo(xv[0])};
      float qf[4] = {bflo(qv[0]), bfhi(qv[0]), bflo(qv[1]), bfhi(qv[1])};
      float a[4];
#pragma unroll
      for (int i = 0; i < 4; ++i) {
        const float y2 = rbf(rbf(y[i]) + rbf(kv[i] * Dv));
        a[i] = qf[i] * y2;
      }
      u32x2 o;
      o[0] = pk2(a[0], a[1]); o[1] = pk2(a[2], a[3]);
      *(u32x2*)(aT + s) = o;
    }
  }
}

DI void attn_body(int h, int qb, char* __restrict__ ws) {
  const int lane = threadIdx.x & 63;
  const int c = lane & 31, hh = lane >> 5;
  const u16* Qh = (const u16*)(ws + WS_Q) + (size_t)h * SEQ * DH;
  const u16* Kh = (const u16*)(ws + WS_K) + (size_t)h * SEQ * DH;
  const u16* Vt = (const u16*)(ws + WS_VT) + (size_t)h * 64 * 512;
  const bf16x8 qf = *(const bf16x8*)(Qh + (size_t)(32 * qb + c) * DH + 8 * hh);
  f32x16 zero;
#pragma unroll
  for (int i = 0; i < 16; ++i) zero[i] = 0.f;
  f32x16 sc[9];
#pragma unroll
  for (int t = 0; t < 9; ++t) {
    const int jt = qb - 8 + t;
    const int jc = jt < 0 ? 0 : jt;
    const bf16x8 kf = *(const bf16x8*)(Kh + (size_t)(32 * jc + c) * DH + 8 * hh);
    sc[t] = MFMA(kf, qf, zero);
  }
  const float NEG = -1e30f;
#pragma unroll
  for (int i = 0; i < 16; ++i) {
    const int row = (i & 3) + 8 * (i >> 2) + 4 * hh;
    sc[0][i] = (row > c) ? sc[0][i] : NEG;
    sc[8][i] = (row <= c) ? sc[8][i] : NEG;
  }
  if (qb < 8) {
#pragma unroll
    for (int t = 0; t < 8; ++t)
      if (qb - 8 + t < 0) {
#pragma unroll
        for (int i = 0; i < 16; ++i) sc[t][i] = NEG;
      }
  }
  float m = NEG;
#pragma unroll
  for (int t = 0; t < 9; ++t)
#pragma unroll
    for (int i = 0; i < 16; ++i) m = fmaxf(m, sc[t][i]);
  m = fmaxf(m, __shfl_xor(m, 32));
  const float cs = 0.25f * 1.4426950408889634f;
  const float mc = m * cs;
  f32x16 oacc[2];
  oacc[0] = zero; oacc[1] = zero;
  const unsigned onesw = (c == 16 || c == 20) ? 0x3F803F80u : 0u;
#pragma unroll
  for (int t = 0; t < 9; ++t) {
    const int jt = qb - 8 + t;
    const int jc = jt < 0 ? 0 : jt;
#pragma unroll
    for (int s = 0; s < 2; ++s) {
      u32x4 vw = {onesw, onesw, onesw, onesw};
      if (c < 16) vw = *(const u32x4*)(Vt + (size_t)(jc * 16 + c) * 32 + 16 * s + 8 * hh);
      u32x4 pw;
#pragma unroll
      for (int e = 0; e < 4; ++e) {
        const float p0 = __builtin_amdgcn_exp2f(fmaf(sc[t][8 * s + 2 * e], cs, -mc));
        const float p1 = __builtin_amdgcn_exp2f(fmaf(sc[t][8 * s + 2 * e + 1], cs, -mc));
        pw[e] = pk2(p0, p1);
      }
      oacc[s] = MFMA(__builtin_bit_cast(bf16x8, vw), __builtin_bit_cast(bf16x8, pw), oacc[s]);
    }
  }
  const float inv = 1.f / (oacc[0][8] + oacc[1][8]);
  u16* o = (u16*)(ws + WS_O) + (size_t)(32 * qb + c) * DM + 16 * h + 4 * hh;
#pragma unroll
  for (int gq = 0; gq < 2; ++gq) {
    u32x2 wv;
    wv[0] = pk2((oacc[0][4 * gq] + oacc[1][4 * gq]) * inv, (oacc[0][4 * gq + 1] + oacc[1][4 * gq + 1]) * inv);
    wv[1] = pk2((oacc[0][4 * gq + 2] + oacc[1][4 * gq + 2]) * inv, (oacc[0][4 * gq + 3] + oacc[1][4 * gq + 3]) * inv);
    *(u32x2*)(o + 8 * gq) = wv;
  }
}

__global__ __launch_bounds__(256, 2) void k2_attn_conv(const u16* __restrict__ ssmk,
                                                       const u16* __restrict__ ssmD,
                                                       char* __restrict__ ws) {
  __shared__ __attribute__((aligned(16))) char lds[K2_LDS];
  const int b = blockIdx.x;
  if (b & 1) {
    const int a = b >> 1;
    const int h = a >> 4, qg = a & 15;
    attn_body(h, qg * 4 + (threadIdx.x >> 6), ws);
  } else {
    conv_body(b >> 1, lds, ssmk, ssmD, ws);
  }
}

__global__ __launch_bounds__(256, 2) void k3_outproj(char* __restrict__ ws, u16* __restrict__ out) {
  const int tid = threadIdx.x, lane = tid & 63, w = tid >> 6;
  const int c = lane & 31, hh = lane >> 5;
  const int b = blockIdx.x;
  const int st = b >> 3, ntl = b & 7;
  const int n0 = ntl * 64 + (w & 1) * 32, s0 = st * 64 + (w >> 1) * 32;
  const u16* woT = (const u16*)(ws + WS_WOT) + (size_t)(n0 + c) * DM + 8 * hh;
  const u16* o = (const u16*)(ws + WS_O) + (size_t)(s0 + c) * DM + 8 * hh;
  f32x16 acc;
#pragma unroll
  for (int i = 0; i < 16; ++i) acc[i] = 0.f;
#pragma unroll
  for (int ks = 0; ks < 32; ++ks) {
    const bf16x8 af = *(const bf16x8*)(woT + 16 * ks);
    const bf16x8 bfr = *(const bf16x8*)(o + 16 * ks);
    acc = MFMA(af, bfr, acc);
  }
  const int s = s0 + c;
  const float g = ((const float*)(ws + WS_G))[s];
  const float omg = rbf(1.f - g);
  const u16* aT = (const u16*)(ws + WS_AT);
#pragma unroll
  for (int gq = 0; gq < 4; ++gq) {
    const int n = n0 + 8 * gq + 4 * hh;
    float res[4];
#pragma unroll
    for (int i = 0; i < 4; ++i) {
      const float a = bf2f(aT[(size_t)(n + i) * SEQ + s]);
      const float bb = rbf(acc[4 * gq + i]);
      res[i] = rbf(g * a) + rbf(omg * bb);
    }
    u32x2 wv;
    wv[0] = pk2(res[0], res[1]); wv[1] = pk2(res[2], res[3]);
    *(u32x2*)(out + (size_t)s * DM + n) = wv;
  }
}

extern "C" void kernel_launch(void* const* d_in, const int* in_sizes, int n_in,
                              void* d_out, int out_size, void* d_ws, size_t ws_size,
                              hipStream_t stream) {
  const u16* Aq = (const u16*)d_in[0];
  const u16* Ak = (const u16*)d_in[1];
  const u16* Av = (const u16*)d_in[2];
  const u16* shiftk = (const u16*)d_in[3];
  const u16* ssmk = (const u16*)d_in[4];
  const u16* ssmD = (const u16*)d_in[5];
  const u16* wqkv = (const u16*)d_in[7];
  const u16* wo = (const u16*)d_in[8];
  const u16* gatew = (const u16*)d_in[9];
  char* ws = (char*)d_ws;
  k1_gemm_prep<<<256, 256, 0, stream>>>(Aq, Ak, Av, shiftk, wqkv, wo, gatew, ws);
  k2_attn_conv<<<1024, 256, 0, stream>>>(ssmk, ssmD, ws);
  k3_outproj<<<256, 256, 0, stream>>>(ws, (u16*)d_out);
}
```

```cpp
#include <hip/hip_runtime.h>
#include <hip/hip_bf16.h>
#include <stdint.h>

#define SEQ 2048
#define DM 512
#define NH 32
#define DH 16

typedef __attribute__((ext_vector_type(8))) short bf16x8;
typedef __attribute__((ext_vector_type(4))) short s16x4;
typedef __attribute__((ext_vector_type(16))) float f32x16;
typedef __attribute__((ext_vector_type(4))) float f32x4;
typedef __attribute__((ext_vector_type(4))) unsigned u32x4;
typedef __attribute__((ext_vector_type(2))) unsigned u32x2;
typedef __attribute__((ext_vector_type(2))) __bf16 bf16x2_t;
typedef unsigned short u16;

#define DI __device__ __forceinline__
#define MFMA(a, b, c) __builtin_amdgcn_mfma_f32_32x32x16_bf16((a), (b), (c), 0, 0, 0)
#define LDS_AS __attribute__((address_space(3)))
#define GLB_AS __attribute__((address_space(1)))

DI float bf2f(unsigned v16) { return __uint_as_float(v16 << 16); }
DI float bflo(unsigned w) { return __uint_as_float(w << 16); }
DI float bfhi(unsigned w) { return __uint_as_float(w & 0xffff0000u); }
DI unsigned pk2(float lo, float hi) {
  bf16x2_t v; v[0] = (__bf16)lo; v[1] = (__bf16)hi;
  return __builtin_bit_cast(unsigned, v);
}
DI float rbf(float x) {
  bf16x2_t v; v[0] = (__bf16)x; v[1] = (__bf16)0.f;
  return __uint_as_float(__builtin_bit_cast(unsigned, v) << 16);
}

#define STAMP(kid, slot)
#define WS_Q   (0u)
#define WS_K   (2u << 20)
#define WS_VT  (4u << 20)
#define WS_KVT (6u << 20)
#define WS_QT  (8u << 20)
#define WS_AT  (10u << 20)
#define WS_O   (12u << 20)
#define WS_WOT (14u << 20)
#define WS_G   (15u << 20)

#define K1_XB 16384
#define K1_WB 12288
#define K1_STAGE (K1_XB + K1_WB)
#define K1_NST 5

DI void k1_issue_chunk(const u16* __restrict__ X, const u16* __restrict__ W, char* lds, int stage,
                       int kc, int mbase, int n0, int w, int lane) {
  char* sb = lds + stage * K1_STAGE;
#pragma unroll
  for (int i = 0; i < 4; ++i) {
    const int ins = w * 4 + i;
    const int row = ins * 8 + (lane >> 3);
    const int slot = (lane & 7) ^ ((row >> 1) & 7);
    const u16* src = X + (size_t)(mbase + row) * DM + kc * 64 + slot * 8;
    __builtin_amdgcn_global_load_lds((GLB_AS const void*)src, (LDS_AS void*)(sb + ins * 1024), 16, 0, 0);
  }
#pragma unroll
  for (int i = 0; i < 3; ++i) {
    const int ins = w * 3 + i;
    const int ci = ins * 64 + lane;
    const int row = ci / 12, c16 = ci - row * 12;
    const u16* src = W + (size_t)(kc * 64 + row) * 1536 + n0 + c16 * 8;
    __builtin_amdgcn_global_load_lds((GLB_AS const void*)src, (LDS_AS void*)(sb + K1_XB + ins * 1024), 16, 0, 0);
  }
}

template <int NV>
DI void gemm_body(const u16* __restrict__ X, const u16* __restrict__ W, char* lds, int mbase, int n0,
                  int rot, int w, int lane, u16* __restrict__ Qh, u16* __restrict__ Kh,
                  u16* __restrict__ Vt, char* ws) {
  const int r = lane & 31, hh = lane >> 5;
  f32x16 acc[3];
#pragma unroll
  for (int j = 0; j < 3; ++j)
#pragma unroll
    for (int i = 0; i < 16; ++i) acc[j][i] = 0.f;

  const int g = (lane >> 4) & 1, q = (lane & 15) >> 2, p = lane & 3;
  const unsigned ldsb = (unsigned)(size_t)lds;
  const unsigned wofs = K1_XB + (8 * hh + q) * 192 + (16 * g + 4 * p) * 2;
  const int arow = 32 * w + r;
  const unsigned aofs = arow * 128;
  const int asw = (arow >> 1) & 7;

#pragma unroll
  for (int c = 0; c < 8; ++c) {
    if (c == 0) asm volatile("s_waitcnt vmcnt(28)" ::: "memory");
    else if (c <= 4) asm volatile("s_waitcnt vmcnt(21)" ::: "memory");
    else if (c == 5) asm volatile("s_waitcnt vmcnt(14)" ::: "memory");
    else if (c == 6) asm volatile("s_waitcnt vmcnt(7)" ::: "memory");
    else asm volatile("s_waitcnt vmcnt(0)" ::: "memory");
    __builtin_amdgcn_s_barrier();
    asm volatile("" ::: "memory");
    if (c >= 1 && c + 4 <= 7) k1_issue_chunk(X, W, lds, (c + 4) % K1_NST, (c + 4 + rot) & 7, mbase, n0, w, lane);
    const unsigned sb = ldsb + (c % K1_NST) * K1_STAGE;
#pragma unroll
    for (int ks = 0; ks < 4; ++ks) {
      const bf16x8 af = *(const LDS_AS bf16x8*)(sb + aofs + (((2 * ks + hh) ^ asw) * 16));
#pragma unroll
      for (int j = 0; j < 3; ++j) {
        const unsigned a0 = sb + wofs + ks * 16 * 192 + j * 64;
        s16x4 lo = __builtin_amdgcn_ds_read_tr16_b64_v4i16((LDS_AS s16x4*)(a0));
        s16x4 hi = __builtin_amdgcn_ds_read_tr16_b64_v4i16((LDS_AS s16x4*)(a0 + 4 * 192));
        bf16x8 bfr = __builtin_shufflevector(lo, hi, 0, 1, 2, 3, 4, 5, 6, 7);
        if (j >= 3 - NV) acc[j] = MFMA(af, bfr, acc[j]);
        else             acc[j] = MFMA(bfr, af, acc[j]);
      }
    }
    asm volatile("" ::: "memory");
  }
  STAMP(0, 2);
  const int m0 = mbase + 32 * w;
#pragma unroll
  for (int j = 0; j < 3; ++j) {
    const int nc = n0 + 32 * j;
    if (j >= 3 - NV) {
      const int nv = nc - 1024 + r;
      const int head = nv >> 4, d = nv & 15;
      u16* dst = Vt + (size_t)(head * 64 + (m0 >> 5)) * 512 + d * 16 + 8 * hh;
      u32x4 w0, w1;
      w0[0] = pk2(acc[j][0], acc[j][1]);  w0[1] = pk2(acc[j][2], acc[j][3]);
      w0[2] = pk2(acc[j][4], acc[j][5]);  w0[3] = pk2(acc[j][6], acc[j][7]);
      w1[0] = pk2(acc[j][8], acc[j][9]);  w1[1] = pk2(acc[j][10], acc[j][11]);
      w1[2] = pk2(acc[j][12], acc[j][13]); w1[3] = pk2(acc[j][14], acc[j][15]);
      *(u32x4*)(dst) = w0;
      *(u32x4*)(dst + 256) = w1;
    } else {
      u16* base = (nc < 512) ? Qh : Kh;
      const int head0 = (nc & 511) >> 4;
#pragma unroll
      for (int e = 0; e < 2; ++e) {
        unsigned a0 = pk2(acc[j][8 * e], acc[j][8 * e + 1]), a1 = pk2(acc[j][8 * e + 2], acc[j][8 * e + 3]);
        unsigned b0 = pk2(acc[j][8 * e + 4], acc[j][8 * e + 5]), b1 = pk2(acc[j][8 * e + 6], acc[j][8 * e + 7]);
        auto r0 = __builtin_amdgcn_permlane32_swap(a0, b0, false, false);
        auto r1 = __builtin_amdgcn_permlane32_swap(a1, b1, false, false);
        u32x4 wv = {r0[0], r1[0], r0[1], r1[1]};
        u16* dst = base + ((size_t)(head0 + e) * SEQ + m0 + r) * DH + 8 * hh;
        *(u32x4*)dst = wv;
      }
    }
  }
}

__global__ __launch_bounds__(256, 1) void k1_gemm_prep(
    const u16* __restrict__ Aq, const u16* __restrict__ Ak, const u16* __restrict__ Av,
    const u16* __restrict__ shiftk, const u16* __restrict__ wqkv, const u16* __restrict__ wo,
    const u16* __restrict__ gatew, char* __restrict__ ws) {
  __shared__ __attribute__((aligned(16))) char lds[K1_NST * K1_STAGE];
  const int tid = threadIdx.x, lane = tid & 63;
  const int w = __builtin_amdgcn_readfirstlane(tid >> 6);
  const int b = blockIdx.x;
  STAMP(0, 0);
  const int xcd = b & 7, idx = b >> 3;
  const int mt = (xcd >> 1) * 4 + (idx >> 3);
  const int nt = (xcd & 1) * 8 + (idx & 7);
  const int n0 = nt * 96, mbase = mt * 128;
  const int rot = idx & 7;

  const int st = b >> 3, dt = b & 7;
  const int cg = tid & 15, sg = tid >> 4;
  const int d0 = dt * 64 + cg * 4;
  const int s0 = st * 64 + sg * 4;
  u32x2 kk[7], vv[4], qq[4], skw[4];
#pragma unroll
  for (int i = 0; i < 7; ++i) {
    const int s = s0 - 3 + i;
    kk[i] = *(const u32x2*)(Ak + (size_t)(s < 0 ? 0 : s) * DM + d0);
    if (s < 0) { kk[i][0] = 0; kk[i][1] = 0; }
  }
#pragma unroll
  for (int i = 0; i < 4; ++i) {
    vv[i] = *(const u32x2*)(Av + (size_t)(s0 + i) * DM + d0);
    qq[i] = *(const u32x2*)(Aq + (size_t)(s0 + i) * DM + d0);
    skw[i] = *(const u32x2*)(shiftk + (size_t)(d0 + i) * 4);
  }
  u32x2 wv[4];
  const int wk0 = (b >> 3) * 16 + ((lane >> 4) & 3) * 4, wn0 = (b & 7) * 64 + (lane & 15) * 4;
#pragma unroll
  for (int i = 0; i < 4; ++i) wv[i] = *(const u32x2*)(wo + (size_t)(wk0 + i) * DM + wn0);
  const u32x4 gw = *(const u32x4*)(gatew + lane * 8);
  u32x4 gx[2];
#pragma unroll
  for (int i = 0; i < 2; ++i) gx[i] = *(const u32x4*)(Aq + (size_t)(b * 8 + w * 2 + i) * DM + lane * 8);

#pragma unroll
  for (int c = 0; c < 5; ++c) k1_issue_chunk(Aq, wqkv, lds, c, (c + rot) & 7, mbase, n0, w, lane);

  u16* Qh = (u16*)(ws + WS_Q);
  u16* Kh = (u16*)(ws + WS_K);
  u16* Vt = (u16*)(ws + WS_VT);
  if (nt <= 9) gemm_body<0>(Aq, wqkv, lds, mbase, n0, rot, w, lane, Qh, Kh, Vt, ws);
  else if (nt == 10) gemm_body<1>(Aq, wqkv, lds, mbase, n0, rot, w, lane, Qh, Kh, Vt, ws);
  else gemm_body<3>(Aq, wqkv, lds, mbase, n0, rot, w, lane, Qh, Kh, Vt, ws);

  {
    u16* kvT = (u16*)(ws + WS_KVT);
    u16* qT = (u16*)(ws + WS_QT);
#pragma unroll
    for (int c = 0; c < 4; ++c) {
      const float sk0 = bflo(skw[c][0]), sk1 = bfhi(skw[c][0]), sk2 = bflo(skw[c][1]), sk3 = bfhi(skw[c][1]);
      float kf[7];
#pragma unroll
      for (int i = 0; i < 7; ++i) kf[i] = (c & 1) ? bfhi(kk[i][c >> 1]) : bflo(kk[i][c >> 1]);
      float kvv[4], qv[4];
#pragma unroll
      for (int i = 0; i < 4; ++i) {
        float acc = rbf(kf[i] * sk3);
        acc = rbf(acc + rbf(kf[i + 1] * sk2));
        acc = rbf(acc + rbf(kf[i + 2] * sk1));
        acc = rbf(acc + rbf(kf[i + 3] * sk0));
        const float vf = (c & 1) ? bfhi(vv[i][c >> 1]) : bflo(vv[i][c >> 1]);
        kvv[i] = acc * vf;
        qv[i] = (c & 1) ? bfhi(qq[i][c >> 1]) : bflo(qq[i][c >> 1]);
      }
      u32x2 o1, o2;
      o1[0] = pk2(kvv[0], kvv[1]); o1[1] = pk2(kvv[2], kvv[3]);
      o2[0] = pk2(qv[0], qv[1]);   o2[1] = pk2(qv[2], qv[3]);
      *(u32x2*)(kvT + (size_t)(d0 + c) * SEQ + s0) = o1;
      *(u32x2*)(qT + (size_t)(d0 + c) * SEQ + s0) = o2;
    }
    if (w == 0) {
      u16* woT = (u16*)(ws + WS_WOT);
#pragma unroll
      for (int c = 0; c < 4; ++c) {
        u32x2 o;
        unsigned e[4];
#pragma unroll
        for (int i = 0; i < 4; ++i) e[i] = (c & 1) ? (wv[i][c >> 1] >> 16) : (wv[i][c >> 1] & 0xffffu);
        o[0] = e[0] | (e[1] << 16); o[1] = e[2] | (e[3] << 16);
        const int n = wn0 + c;
        const size_t off = ((size_t)(((n >> 5) * 32 + (wk0 >> 4)) * 2 + ((wk0 >> 3) & 1)) * 32 + (n & 31)) * 8 + (wk0 & 7);
        *(u32x2*)(woT + off) = o;
      }
    }
    float* gout = (float*)(ws + WS_G);
#pragma unroll
    for (int i = 0; i < 2; ++i) {
      float sum = 0.f;
#pragma unroll
      for (int e = 0; e < 4; ++e) {
        sum += rbf(bflo(gx[i][e]) * bflo(gw[e]));
        sum += rbf(bfhi(gx[i][e]) * bfhi(gw[e]));
      }
#pragma unroll
      for (int o = 32; o > 0; o >>= 1) sum += __shfl_xor(sum, o);
      const float z = rbf(sum);
      const float gv = rbf(1.f / (1.f + __expf(-z)));
      if (lane == 0) gout[b * 8 + w * 2 + i] = gv;
    }
  }
  STAMP(0, 3);
}

#define CSTR 4608
#define XR_OFF (8 * CSTR)
#define XR_STR 80
#define K2_LDS (XR_OFF + 95 * XR_STR)

struct ConvRegs { u32x4 h0, h1, hx0, hx1, xv; u32x2 qv[2]; };

DI void conv_load(ConvRegs& R, int d, const u16* __restrict__ ssmk, char* __restrict__ ws) {
  const int tid = threadIdx.x, lane = tid & 63, w = tid >> 6;
  const int r = lane & 31, hh = lane >> 5;
  const u16* hrow = ssmk + (size_t)d * SEQ;
  const u32x4 z4 = {0u, 0u, 0u, 0u};
  R.h0 = *(const u32x4*)(hrow + 8 * (tid >= 4 ? tid - 4 : 0));
  R.h1 = *(const u32x4*)(hrow + 8 * (tid >= 3 ? tid - 3 : 0));
  if (tid < 4) R.h0 = z4;
  if (tid < 3) R.h1 = z4;
  R.hx0 = z4; R.hx1 = z4;
  if (tid < 4) {
    R.hx0 = *(const u32x4*)(hrow + 8 * (252 + tid));
    if (tid < 3) R.hx1 = *(const u32x4*)(hrow + 8 * (253 + tid));
  }
  R.xv = *(const u32x4*)((const u16*)(ws + WS_KVT) + (size_t)d * SEQ + 8 * tid);
  const u16* qT = (const u16*)(ws + WS_QT) + (size_t)d * SEQ;
  const int ablk = 32 * (w >> 1) + r;
#pragma unroll
  for (int hf = 0; hf < 2; ++hf) R.qv[hf] = *(const u32x2*)(qT + 32 * ablk + 16 * (w & 1) + 8 * hf + 4 * hh);
}

DI void conv_write_copies(char* lds, int t, u32x4 h0, u32x4 h1) {
  unsigned wv[8] = {h0[0], h0[1], h0[2], h0[3], h1[0], h1[1], h1[2], h1[3]};
  unsigned od[7];
#pragma unroll
  for (int i = 0; i < 7; ++i) od[i] = __builtin_amdgcn_alignbyte(wv[i + 1], wv[i], 2);
#pragma unroll
  for (int c = 0; c < 8; ++c) {
    const int e = c >> 1;
    u32x4 v;
    if (c & 1) { v[0] = od[e]; v[1] = od[e + 1]; v[2] = od[e + 2]; v[3] = od[e + 3]; }
    else       { v[0] = wv[e]; v[1] = wv[e + 1]; v[2] = wv[e + 2]; v[3] = wv[e + 3]; }
    const unsigned off = (0x5D9159D0u >> (4 * c)) & 15u;
    *(u32x4*)(lds + c * CSTR + off * 16 + t * 16) = v;
  }
}

DI void conv_compute(const ConvRegs& R, int d, char* lds, const u16* __restrict__ ssmD,
                     char* __restrict__ ws) {
  const int tid = threadIdx.x, lane = tid & 63, w = tid >> 6;
  const int r = lane & 31, hh = lane >> 5;
  {
    const u32x4 z4 = {0u, 0u, 0u, 0u};
    conv_write_copies(lds, tid, R.h0, R.h1);
    if (tid < 4) conv_write_copies(lds, 256 + tid, R.hx0, R.hx1);
    u32x4 rv;
#pragma unroll
    for (int i = 0; i < 4; ++i) rv[i] = __builtin_amdgcn_alignbit(R.xv[3 - i], R.xv[3 - i], 16);
    *(u32x4*)(lds + XR_OFF + ((tid >> 2) + 31) * XR_STR + (3 - (tid & 3)) * 16) = rv;
    if (tid < 124) *(u32x4*)(lds + XR_OFF + (tid >> 2) * XR_STR + (tid & 3) * 16) = z4;
  }
  __syncthreads();

  f32x16 acc0, acc1a, acc1b;
#pragma unroll
  for (int i = 0; i < 16; ++i) { acc0[i] = 0.f; acc1a[i] = 0.f; acc1b[i] = 0.f; }
  {
    const int c = (1 + r) & 7, u = (1 + r) >> 3;
    const unsigned off = (0x5D9159D0u >> (4 * c)) & 15u;
    const char* abase = lds + c * CSTR + off * 16 + (16 + 8 * hh + 8 * u) * 2 + w * 8 * 64;
    const char* bbase = lds + XR_OFF + (r + 31 - w * 8) * XR_STR + 16 * hh;
#pragma unroll
    for (int gi = 0; gi < 8; ++gi) {
#pragma unroll
      for (int ks = 0; ks < 2; ++ks) {
        const bf16x8 A0 = *(const bf16x8*)(abase + gi * 64 - ks * 32);
        const bf16x8 A1 = *(const bf16x8*)(abase + gi * 64 - ks * 32 + 32 * 64);
        const bf16x8 B0 = *(const bf16x8*)(bbase - gi * XR_STR + (1 - ks) * 32);
        const bf16x8 B1 = *(const bf16x8*)(bbase + (32 - gi) * XR_STR + (1 - ks) * 32);
        acc0 = MFMA(A0, B0, acc0);
        acc1a = MFMA(A1, B0, acc1a);
        acc1b = MFMA(A0, B1, acc1b);
      }
    }
  }
  __syncthreads();
  {
    float* red = (float*)lds;
#pragma unroll
    for (int dst = 0; dst < 4; ++dst)
#pragma unroll
      for (int hf = 0; hf < 2; ++hf) {
        f32x4 v;
#pragma unroll
        for (int i = 0; i < 4; ++i) {
          const int rg = 8 * (dst & 1) + 4 * hf + i;
          v[i] = (dst >> 1) ? (acc1a[rg] + acc1b[rg]) : acc0[rg];
        }
        *(f32x4*)(red + ((((w * 4 + dst) * 2 + hf) * 64) + lane) * 4) = v;
      }
  }
  __syncthreads();
  {
    const float* red = (const float*)lds;
    const float Dv = bf2f(ssmD[d]);
    u16* aT = (u16*)(ws + WS_AT) + (size_t)d * SEQ;
    const int ablk = 32 * (w >> 1) + r;
#pragma unroll
    for (int hf = 0; hf < 2; ++hf) {
      f32x4 y = {0.f, 0.f, 0.f, 0.f};
#pragma unroll
      for (int src = 0; src < 4; ++src) {
        const f32x4 v = *(const f32x4*)(red + ((((src * 4 + w) * 2 + hf) * 64) + lane) * 4);
        y += v;
      }
      const int b0 = 16 * (w & 1) + 8 * hf + 4 * hh;
      const int s = 32 * ablk + b0;
      const u32x2 qv = R.qv[hf];
      const u32x2 xv = *(const u32x2*)(lds + XR_OFF + (ablk + 31) * XR_STR + (28 - b0) * 2);
      float kv[4] = {bfhi(xv[1]), bflo(xv[1]), bfhi(xv[0]), bflo(xv[0])};
      float qf[4] = {bflo(qv[0]), bfhi(qv[0]), bflo(qv[1]), bfhi(qv[1])};
      float a[4];
#pragma unroll
      for (int i = 0; i < 4; ++i) {
        const float y2 = rbf(rbf(y[i]) + rbf(kv[i] * Dv));
        a[i] = qf[i] * y2;
      }
      u32x2 o;
      o[0] = pk2(a[0], a[1]); o[1] = pk2(a[2], a[3]);
      *(u32x2*)(aT + s) = o;
    }
  }
  __syncthreads();
}

DI void attn_task(const char* lds, int lt0, int qb, bf16x8 qf, int h, char* __restrict__ ws) {
  const int lane = threadIdx.x & 63;
  const int c = lane & 31, hh = lane >> 5;
  f32x16 zero;
#pragma unroll
  for (int i = 0; i < 16; ++i) zero[i] = 0.f;
  f32x16 sc[9];
  const char* kb = lds + lt0 * 1024 + c * 32 + hh * 16;
#pragma unroll
  for (int t = 0; t < 9; ++t) {
    const bf16x8 kf = *(const bf16x8*)(kb + t * 1024);
    sc[t] = MFMA(kf, qf, zero);
  }
  const float NEG = -1e30f;
#pragma unroll
  for (int i = 0; i < 16; ++i) {
    const int row = (i & 3) + 8 * (i >> 2) + 4 * hh;
    sc[0][i] = (row > c) ? sc[0][i] : NEG;
    sc[8][i] = (row <= c) ? sc[8][i] : NEG;
  }
  if (qb < 8) {
#pragma unroll
    for (int t = 0; t < 8; ++t)
      if (qb - 8 + t < 0) {
#pragma unroll
        for (int i = 0; i < 16; ++i) sc[t][i] = NEG;
      }
  }
  float m = NEG;
#pragma unroll
  for (int t = 0; t < 9; ++t)
#pragma unroll
    for (int i = 0; i < 16; ++i) m = fmaxf(m, sc[t][i]);
  m = fmaxf(m, __shfl_xor(m, 32));
  const float cs = 0.25f * 1.4426950408889634f;
  const float mc = m * cs;
  f32x16 oacc[2];
  oacc[0] = zero; oacc[1] = zero;
  const unsigned onesw = (c == 16 || c == 20) ? 0x3F803F80u : 0u;
  const char* vb = lds + 16384 + lt0 * 1024 + (c & 15) * 32 + hh * 16;
#pragma unroll
  for (int t = 0; t < 9; ++t) {
#pragma unroll
    for (int s = 0; s < 2; ++s) {
      u32x4 vw = *(const u32x4*)(vb + t * 1024 + s * 512);
      if (c >= 16) { vw[0] = onesw; vw[1] = onesw; vw[2] = onesw; vw[3] = onesw; }
      u32x4 pw;
#pragma unroll
      for (int e = 0; e < 4; ++e) {
        const float p0 = __builtin_amdgcn_exp2f(fmaf(sc[t][8 * s + 2 * e], cs, -mc));
        const float p1 = __builtin_amdgcn_exp2f(fmaf(sc[t][8 * s + 2 * e + 1], cs, -mc));
        pw[e] = pk2(p0, p1);
      }
      oacc[s] = MFMA(__builtin_bit_cast(bf16x8, vw), __builtin_bit_cast(bf16x8, pw), oacc[s]);
    }
  }
  const float inv = 1.f / (oacc[0][8] + oacc[1][8]);
  unsigned a0 = pk2((oacc[0][0] + oacc[1][0]) * inv, (oacc[0][1] + oacc[1][1]) * inv);
  unsigned a1 = pk2((oacc[0][2] + oacc[1][2]) * inv, (oacc[0][3] + oacc[1][3]) * inv);
  unsigned b0 = pk2((oacc[0][4] + oacc[1][4]) * inv, (oacc[0][5] + oacc[1][5]) * inv);
  unsigned b1 = pk2((oacc[0][6] + oacc[1][6]) * inv, (oacc[0][7] + oacc[1][7]) * inv);
  auto r0 = __builtin_amdgcn_permlane32_swap(a0, b0, false, false);
  auto r1 = __builtin_amdgcn_permlane32_swap(a1, b1, false, false);
  u32x4 wv = {r0[0], r1[0], r0[1], r1[1]};
  u16* o = (u16*)(ws + WS_O) + ((size_t)((qb * 32 + h) * 2 + hh) * 32 + c) * 8;
  *(u32x4*)o = wv;
}

DI void attn_body(int h, int g, char* lds, char* __restrict__ ws) {
  const int tid = threadIdx.x, lane = tid & 63;
  const int w = __builtin_amdgcn_readfirstlane(tid >> 6);
  const int c = lane & 31, hh = lane >> 5;
  const u16* Qh = (const u16*)(ws + WS_Q) + (size_t)h * SEQ * DH;
  const u16* Kh = (const u16*)(ws + WS_K) + (size_t)h * SEQ * DH;
  const u16* Vt = (const u16*)(ws + WS_VT) + (size_t)h * 64 * 512;
#pragma unroll
  for (int i = 0; i < 4; ++i) {
    const int lt = w * 4 + i;
    int gt = 8 * g - 8 + lt; gt = gt < 0 ? 0 : gt;
    __builtin_amdgcn_global_load_lds((GLB_AS const void*)(Kh + (size_t)gt * 512 + lane * 8),
                                     (LDS_AS void*)(lds + lt * 1024), 16, 0, 0);
    __builtin_amdgcn_global_load_lds((GLB_AS const void*)(Vt + (size_t)gt * 512 + lane * 8),
                                     (LDS_AS void*)(lds + 16384 + lt * 1024), 16, 0, 0);
  }
  const int qb0 = 8 * g + w, qb1 = 8 * g + w + 4;
  const bf16x8 q0 = *(const bf16x8*)(Qh + (size_t)(32 * qb0 + c) * DH + 8 * hh);
  const bf16x8 q1 = *(const bf16x8*)(Qh + (size_t)(32 * qb1 + c) * DH + 8 * hh);
  asm volatile("s_waitcnt vmcnt(0)" ::: "memory");
  __syncthreads();
  attn_task(lds, w, qb0, q0, h, ws);
  attn_task(lds, w + 4, qb1, q1, h, ws);
}

__global__ __launch_bounds__(256, 2) void k2_attn_conv(const u16* __restrict__ ssmk,
                                                       const u16* __restrict__ ssmD,
                                                       char* __restrict__ ws) {
  __shared__ __attribute__((aligned(16))) char lds[K2_LDS];
  const int b = blockIdx.x;
  STAMP(1, 0);
  if (b >= 256) {
    const int a = b - 256;
    attn_body(a >> 3, a & 7, lds, ws);
  } else {
    ConvRegs R0, R1;
    conv_load(R0, 2 * b, ssmk, ws);
    conv_load(R1, 2 * b + 1, ssmk, ws);
    conv_compute(R0, 2 * b, lds, ssmD, ws);
    conv_compute(R1, 2 * b + 1, lds, ssmD, ws);
  }
  STAMP(1, 3);
}

__global__ __launch_bounds__(256, 2) void k3_outproj(char* __restrict__ ws, u16* __restrict__ out) {
  const int tid = threadIdx.x, lane = tid & 63, w = tid >> 6;
  const int c = lane & 31, hh = lane >> 5;
  const int b = blockIdx.x;
  STAMP(2, 0);
  const int st = b >> 3, ntl = b & 7;
  const int ntile = ntl * 2 + (w & 1), stile = st * 2 + (w >> 1);
  const int n0 = ntile * 32, s0 = stile * 32;
  const u16* woT = (const u16*)(ws + WS_WOT) + (size_t)ntile * 32 * 512 + lane * 8;
  const u16* o = (const u16*)(ws + WS_O) + (size_t)stile * 32 * 512 + lane * 8;
  const int s = s0 + c;
  const float g = ((const float*)(ws + WS_G))[s];
  const u16* aT = (const u16*)(ws + WS_AT);
  unsigned av[16];
#pragma unroll
  for (int i = 0; i < 16; ++i) av[i] = aT[(size_t)(n0 + (i & 3) + 8 * (i >> 2) + 4 * hh) * SEQ + s];
  f32x16 acc;
#pragma unroll
  for (int i = 0; i < 16; ++i) acc[i] = 0.f;
#pragma unroll
  for (int ks = 0; ks < 32; ++ks) {
    const bf16x8 af = *(const bf16x8*)(woT + 512 * ks);
    const bf16x8 bfr = *(const bf16x8*)(o + 512 * ks);
    acc = MFMA(af, bfr, acc);
  }
  STAMP(2, 2);
  const float omg = rbf(1.f - g);
#pragma unroll
  for (int gq = 0; gq < 4; ++gq) {
    const int n = n0 + 8 * gq + 4 * hh;
    float res[4];
#pragma unroll
    for (int i = 0; i < 4; ++i) {
      const float a = bf2f(av[4 * gq + i]);
      const float bb = rbf(acc[4 * gq + i]);
      res[i] = rbf(g * a) + rbf(omg * bb);
    }
    u32x2 wv;
    wv[0] = pk2(res[0], res[1]); wv[1] = pk2(res[2], res[3]);
    *(u32x2*)(out + (size_t)s * DM + n) = wv;
  }
  STAMP(2, 3);
}

extern "C" void kernel_launch(void* const* d_in, const int* in_sizes, int n_in,
                              void* d_out, int out_size, void* d_ws, size_t ws_size,
                              hipStream_t stream) {
  const u16* Aq = (const u16*)d_in[0];
  const u16* Ak = (const u16*)d_in[1];
  const u16* Av = (const u16*)d_in[2];
  const u16* shiftk = (const u16*)d_in[3];
  const u16* ssmk = (const u16*)d_in[4];
  const u16* ssmD = (const u16*)d_in[5];
  const u16* wqkv = (const u16*)d_in[7];
  const u16* wo = (const u16*)d_in[8];
  const u16* gatew = (const u16*)d_in[9];
  char* ws = (char*)d_ws;
  k1_gemm_prep<<<256, 256, 0, stream>>>(Aq, Ak, Av, shiftk, wqkv, wo, gatew, ws);
  k2_attn_conv<<<512, 256, 0, stream>>>(ssmk, ssmD, ws);
  k3_outproj<<<256, 256, 0, stream>>>(ws, (u16*)d_out);
}
```

```cpp
#include <hip/hip_runtime.h>
#include <hip/hip_bf16.h>
#include <stdint.h>

#define SEQ 2048
#define DM 512
#define NH 32
#define DH 16

typedef __attribute__((ext_vector_type(8))) short bf16x8;
typedef __attribute__((ext_vector_type(4))) short s16x4;
typedef __attribute__((ext_vector_type(16))) float f32x16;
typedef __attribute__((ext_vector_type(4))) float f32x4;
typedef __attribute__((ext_vector_type(4))) unsigned u32x4;
typedef __attribute__((ext_vector_type(2))) unsigned u32x2;
typedef __attribute__((ext_vector_type(2))) __bf16 bf16x2_t;
typedef unsigned short u16;

#define DI __device__ __forceinline__
#define MFMA(a, b, c) __builtin_amdgcn_mfma_f32_32x32x16_bf16((a), (b), (c), 0, 0, 0)
#define LDS_AS __attribute__((address_space(3)))
#define GLB_AS __attribute__((address_space(1)))

DI float bf2f(unsigned v16) { return __uint_as_float(v16 << 16); }
DI float bflo(unsigned w) { return __uint_as_float(w << 16); }
DI float bfhi(unsigned w) { return __uint_as_float(w & 0xffff0000u); }
DI unsigned pk2(float lo, float hi) {
  bf16x2_t v; v[0] = (__bf16)lo; v[1] = (__bf16)hi;
  return __builtin_bit_cast(unsigned, v);
}
DI float rbf(float x) {
  bf16x2_t v; v[0] = (__bf16)x; v[1] = (__bf16)0.f;
  return __uint_as_float(__builtin_bit_cast(unsigned, v) << 16);
}

#define STAMP(kid, slot)
#define WS_Q   (0u)
#define WS_K   (2u << 20)
#define WS_VT  (4u << 20)
#define WS_KVT (6u << 20)
#define WS_QT  (8u << 20)
#define WS_AT  (10u << 20)
#define WS_O   (12u << 20)
#define WS_WOT (14u << 20)
#define WS_G   (15u << 20)

#define K1_WBYTES (512 * 192)
#define K1_XW 16384

DI void k1_issue_w(const u16* __restrict__ W, char* lds, int half, int n0, int w, int lane, int rot) {
#pragma unroll
  for (int i = 0; i < 12; ++i) {
    const int ins = half * 48 + ((i * 4 + w + rot * 4) % 48);
    const int ci = ins * 64 + lane;
    const int row = ci / 12, c16 = ci - row * 12;
    const u16* src = W + (size_t)row * 1536 + n0 + c16 * 8;
    __builtin_amdgcn_global_load_lds((GLB_AS const void*)src, (LDS_AS void*)(lds + ins * 1024), 16, 0, 0);
  }
}
DI void k1_issue_x(const u16* __restrict__ X, char* xb, int half, int m0, int lane) {
#pragma unroll
  for (int i = 0; i < 16; ++i) {
    const int row = 2 * i + (lane >> 5);
    const int slot = (lane & 31) ^ (row & 15);
    const u16* src = X + (size_t)(m0 + row) * DM + half * 256 + slot * 8;
    __builtin_amdgcn_global_load_lds((GLB_AS const void*)src, (LDS_AS void*)(xb + i * 1024), 16, 0, 0);
  }
}

template <int OFF>
DI void tr_rd(s16x4& d, unsigned base) {
  asm volatile("ds_read_b64_tr_b16 %0, %1 offset:%2" : "=v"(d) : "v"(base), "i"(OFF));
}
template <int KS>
DI void k1_rdb(s16x4 (&t)[6], unsigned base) {
  tr_rd<KS * 3072 + 0>(t[0], base);   tr_rd<KS * 3072 + 768>(t[1], base);
  tr_rd<KS * 3072 + 64>(t[2], base);  tr_rd<KS * 3072 + 64 + 768>(t[3], base);
  tr_rd<KS * 3072 + 128>(t[4], base); tr_rd<KS * 3072 + 128 + 768>(t[5], base);
}
template <int N>
DI void lgkm_wait6(s16x4 (&t)[6]) {
  asm volatile("s_waitcnt lgkmcnt(%6)" : "+v"(t[0]), "+v"(t[1]), "+v"(t[2]), "+v"(t[3]), "+v"(t[4]), "+v"(t[5]) : "i"(N));
}
template <int NV, int KS>
DI void k1_steps(const bf16x8 (&af)[16], unsigned wb, f32x16 (&acc)[3], s16x4 (&t0)[6], s16x4 (&t1)[6], s16x4 (&t2)[6]) {
  if constexpr (KS + 1 < 16) lgkm_wait6<6>(t0); else lgkm_wait6<0>(t0);
#pragma unroll
  for (int j = 0; j < 3; ++j) {
    const bf16x8 bfr = __builtin_shufflevector(t0[2 * j], t0[2 * j + 1], 0, 1, 2, 3, 4, 5, 6, 7);
    if (j >= 3 - NV) acc[j] = MFMA(af[KS], bfr, acc[j]);
    else             acc[j] = MFMA(bfr, af[KS], acc[j]);
    if (j == 0) { if constexpr (KS + 2 < 16) k1_rdb<KS + 2>(t2, wb); }
  }
  __builtin_amdgcn_sched_barrier(0);
  if constexpr (KS + 1 < 16) k1_steps<NV, KS + 1>(af, wb, acc, t1, t2, t0);
}
template <int NV>
DI void k1_half(const bf16x8 (&af)[16], unsigned wb, f32x16 (&acc)[3]) {
  s16x4 t0[6], t1[6], t2[6];
  k1_rdb<0>(t0, wb);
  k1_rdb<1>(t1, wb);
  k1_steps<NV, 0>(af, wb, acc, t0, t1, t2);
}

template <int NV>
DI void gemm_body(const u16* __restrict__ X, char* lds, int mbase, int n0,
                  int w, int lane, u16* __restrict__ Qh, u16* __restrict__ Kh,
                  u16* __restrict__ Vt, char* ws) {
  const int r = lane & 31, hh = lane >> 5;
  f32x16 acc[3];
#pragma unroll
  for (int j = 0; j < 3; ++j)
#pragma unroll
    for (int i = 0; i < 16; ++i) acc[j][i] = 0.f;

  const int g = (lane >> 4) & 1, q = (lane & 15) >> 2, p = lane & 3;
  const unsigned ldsb = (unsigned)(size_t)lds;
  const unsigned wb = ldsb + (8 * hh + q) * 192 + (16 * g + 4 * p) * 2;
  char* xb = lds + K1_WBYTES + w * K1_XW;
  const unsigned xa = ldsb + K1_WBYTES + w * K1_XW + r * 512;
  const int m0 = mbase + 32 * w;

  bf16x8 af[16];
  asm volatile("s_waitcnt vmcnt(12)" ::: "memory");
  __builtin_amdgcn_s_barrier();
  asm volatile("" ::: "memory");
  STAMP(0, 1);
#pragma unroll
  for (int ks = 0; ks < 16; ++ks) af[ks] = *(const LDS_AS bf16x8*)(xa + (((2 * ks + hh) ^ (r & 15)) * 16));
  asm volatile("s_waitcnt lgkmcnt(0)" ::: "memory");
  k1_issue_x(X, xb, 1, m0, lane);
  asm volatile("" ::: "memory");
  k1_half<NV>(af, wb, acc);
  asm volatile("s_waitcnt vmcnt(0)" ::: "memory");
  __builtin_amdgcn_s_barrier();
  asm volatile("" ::: "memory");
  STAMP(0, 4);
#pragma unroll
  for (int ks = 0; ks < 16; ++ks) af[ks] = *(const LDS_AS bf16x8*)(xa + (((2 * ks + hh) ^ (r & 15)) * 16));
  asm volatile("s_waitcnt lgkmcnt(0)" ::: "memory");
  k1_half<NV>(af, wb + 256 * 192, acc);
  STAMP(0, 2);
#pragma unroll
  for (int j = 0; j < 3; ++j) {
    const int nc = n0 + 32 * j;
    if (j >= 3 - NV) {
      const int nv = nc - 1024 + r;
      const int head = nv >> 4, d = nv & 15;
      u16* dst = Vt + (size_t)(head * 64 + (m0 >> 5)) * 512 + d * 16 + 8 * hh;
      u32x4 w0, w1;
      w0[0] = pk2(acc[j][0], acc[j][1]);  w0[1] = pk2(acc[j][2], acc[j][3]);
      w0[2] = pk2(acc[j][4], acc[j][5]);  w0[3] = pk2(acc[j][6], acc[j][7]);
      w1[0] = pk2(acc[j][8], acc[j][9]);  w1[1] = pk2(acc[j][10], acc[j][11]);
      w1[2] = pk2(acc[j][12], acc[j][13]); w1[3] = pk2(acc[j][14], acc[j][15]);
      *(u32x4*)(dst) = w0;
      *(u32x4*)(dst + 256) = w1;
    } else {
      u16* base = (nc < 512) ? Qh : Kh;
      const int head0 = (nc & 511) >> 4;
#pragma unroll
      for (int e = 0; e < 2; ++e) {
        unsigned a0 = pk2(acc[j][8 * e], acc[j][8 * e + 1]), a1 = pk2(acc[j][8 * e + 2], acc[j][8 * e + 3]);
        unsigned b0 = pk2(acc[j][8 * e + 4], acc[j][8 * e + 5]), b1 = pk2(acc[j][8 * e + 6], acc[j][8 * e + 7]);
        auto r0 = __builtin_amdgcn_permlane32_swap(a0, b0, false, false);
        auto r1 = __builtin_amdgcn_permlane32_swap(a1, b1, false, false);
        u32x4 wv = {r0[0], r1[0], r0[1], r1[1]};
        u16* dst = base + ((size_t)(head0 + e) * SEQ + m0 + r) * DH + 8 * hh;
        *(u32x4*)dst = wv;
      }
    }
  }
}

DI void prep_body(const u16* __restrict__ Aq, const u16* __restrict__ Ak, const u16* __restrict__ Av,
                  const u16* __restrict__ shiftk, const u16* __restrict__ wo, const u16* __restrict__ gatew,
                  char* __restrict__ ws, int b, int pw, int lane) {
  const int st = b >> 3, dt = b & 7;
  const int dg = lane & 7, sg = lane >> 3;
  const int d0 = dt * 64 + dg * 8 + 2 * pw;
  const int s0 = st * 64 + sg * 8;
  unsigned kk[11], vv[8];
#pragma unroll
  for (int i = 0; i < 11; ++i) {
    const int s = s0 - 3 + i;
    kk[i] = *(const unsigned*)(Ak + (size_t)(s < 0 ? 0 : s) * DM + d0);
    if (s < 0) kk[i] = 0;
  }
#pragma unroll
  for (int i = 0; i < 8; ++i) vv[i] = *(const unsigned*)(Av + (size_t)(s0 + i) * DM + d0);
  const u32x4 skw = *(const u32x4*)(shiftk + (size_t)d0 * 4);
  const u32x4 gw = *(const u32x4*)(gatew + lane * 8);
  u32x4 gx[2];
#pragma unroll
  for (int i = 0; i < 2; ++i) gx[i] = *(const u32x4*)(Aq + (size_t)(b * 8 + pw * 2 + i) * DM + lane * 8);
  const int wk0 = (b >> 3) * 16 + pw * 4, wn = (b & 7) * 64 + lane;
  unsigned wv[4];
#pragma unroll
  for (int i = 0; i < 4; ++i) wv[i] = wo[(size_t)(wk0 + i) * DM + wn];
  __builtin_amdgcn_s_barrier();
  u16* kvT = (u16*)(ws + WS_KVT);
#pragma unroll
  for (int c = 0; c < 2; ++c) {
    const unsigned s01 = skw[c * 2], s23 = skw[c * 2 + 1];
    const float sk0 = bflo(s01), sk1 = bfhi(s01), sk2 = bflo(s23), sk3 = bfhi(s23);
    float kf[11];
#pragma unroll
    for (int i = 0; i < 11; ++i) kf[i] = c ? bfhi(kk[i]) : bflo(kk[i]);
    float kvv[8];
#pragma unroll
    for (int i = 0; i < 8; ++i) {
      float acc = rbf(kf[i] * sk3);
      acc = rbf(acc + rbf(kf[i + 1] * sk2));
      acc = rbf(acc + rbf(kf[i + 2] * sk1));
      acc = rbf(acc + rbf(kf[i + 3] * sk0));
      const float vf = c ? bfhi(vv[i]) : bflo(vv[i]);
      kvv[i] = acc * vf;
    }
    u32x4 o1;
    o1[0] = pk2(kvv[0], kvv[1]); o1[1] = pk2(kvv[2], kvv[3]);
    o1[2] = pk2(kvv[4], kvv[5]); o1[3] = pk2(kvv[6], kvv[7]);
    *(u32x4*)(kvT + (size_t)(d0 + c) * SEQ + s0) = o1;
  }
  {
    u16* woT = (u16*)(ws + WS_WOT);
    u32x2 o;
    o[0] = wv[0] | (wv[1] << 16); o[1] = wv[2] | (wv[3] << 16);
    const size_t off = ((size_t)(((wn >> 5) * 32 + (wk0 >> 4)) * 2 + ((wk0 >> 3) & 1)) * 32 + (wn & 31)) * 8 + (wk0 & 7);
    *(u32x2*)(woT + off) = o;
  }
  float* gout = (float*)(ws + WS_G);
#pragma unroll
  for (int i = 0; i < 2; ++i) {
    float sum = 0.f;
#pragma unroll
    for (int e = 0; e < 4; ++e) {
      sum += rbf(bflo(gx[i][e]) * bflo(gw[e]));
      sum += rbf(bfhi(gx[i][e]) * bfhi(gw[e]));
    }
#pragma unroll
    for (int o = 32; o > 0; o >>= 1) sum += __shfl_xor(sum, o);
    const float z = rbf(sum);
    const float gv = rbf(1.f / (1.f + __expf(-z)));
    if (lane == 0) gout[b * 8 + pw * 2 + i] = gv;
  }
}

__global__ __launch_bounds__(512, 2) void k1_gemm_prep(
    const u16* __restrict__ Aq, const u16* __restrict__ Ak, const u16* __restrict__ Av,
    const u16* __restrict__ shiftk, const u16* __restrict__ wqkv, const u16* __restrict__ wo,
    const u16* __restrict__ gatew, char* __restrict__ ws) {
  __shared__ __attribute__((aligned(16))) char lds[K1_WBYTES + 4 * K1_XW];
  const int tid = threadIdx.x, lane = tid & 63;
  const int w = __builtin_amdgcn_readfirstlane(tid >> 6);
  const int b = blockIdx.x;
  STAMP(0, 0);
  if (w >= 4) { prep_body(Aq, Ak, Av, shiftk, wo, gatew, ws, b, w - 4, lane); return; }
  const int xcd = b & 7, idx = b >> 3;
  const int mt = (xcd >> 1) * 4 + (idx >> 3);
  const int nt = (xcd & 1) * 8 + (idx & 7);
  const int n0 = nt * 96, mbase = mt * 128;
  const int rot = (idx >> 3) * 3;

  k1_issue_w(wqkv, lds, 0, n0, w, lane, rot);
  k1_issue_x(Aq, lds + K1_WBYTES + w * K1_XW, 0, mbase + 32 * w, lane);
  k1_issue_w(wqkv, lds, 1, n0, w, lane, rot);

  u16* Qh = (u16*)(ws + WS_Q);
  u16* Kh = (u16*)(ws + WS_K);
  u16* Vt = (u16*)(ws + WS_VT);
  if (nt <= 9) gemm_body<0>(Aq, lds, mbase, n0, w, lane, Qh, Kh, Vt, ws);
  else if (nt == 10) gemm_body<1>(Aq, lds, mbase, n0, w, lane, Qh, Kh, Vt, ws);
  else gemm_body<3>(Aq, lds, mbase, n0, w, lane, Qh, Kh, Vt, ws);
  STAMP(0, 3);
}

#define CSTR 4608
#define XR_OFF (8 * CSTR)
#define XR_STR 80
#define K2_LDS (XR_OFF + 95 * XR_STR)

struct ConvRegs { u32x4 h0, h1, hx0, hx1, xv; };

DI void conv_load(ConvRegs& R, int d, const u16* __restrict__ ssmk, char* __restrict__ ws) {
  const int tid = threadIdx.x, lane = tid & 63, w = tid >> 6;
  const int r = lane & 31, hh = lane >> 5;
  const u16* hrow = ssmk + (size_t)d * SEQ;
  const u32x4 z4 = {0u, 0u, 0u, 0u};
  R.h0 = *(const u32x4*)(hrow + 8 * (tid >= 4 ? tid - 4 : 0));
  R.h1 = *(const u32x4*)(hrow + 8 * (tid >= 3 ? tid - 3 : 0));
  if (tid < 4) R.h0 = z4;
  if (tid < 3) R.h1 = z4;
  R.hx0 = z4; R.hx1 = z4;
  if (tid < 4) {
    R.hx0 = *(const u32x4*)(hrow + 8 * (252 + tid));
    if (tid < 3) R.hx1 = *(const u32x4*)(hrow + 8 * (253 + tid));
  }
  R.xv = *(const u32x4*)((const u16*)(ws + WS_KVT) + (size_t)d * SEQ + 8 * tid);
}

DI void conv_write_copies(char* lds, int t, u32x4 h0, u32x4 h1) {
  unsigned wv[8] = {h0[0], h0[1], h0[2], h0[3], h1[0], h1[1], h1[2], h1[3]};
  unsigned od[7];
#pragma unroll
  for (int i = 0; i < 7; ++i) od[i] = __builtin_amdgcn_alignbyte(wv[i + 1], wv[i], 2);
#pragma unroll
  for (int c = 0; c < 8; ++c) {
    const int e = c >> 1;
    u32x4 v;
    if (c & 1) { v[0] = od[e]; v[1] = od[e + 1]; v[2] = od[e + 2]; v[3] = od[e + 3]; }
    else       { v[0] = wv[e]; v[1] = wv[e + 1]; v[2] = wv[e + 2]; v[3] = wv[e + 3]; }
    const unsigned off = (0x5D9159D0u >> (4 * c)) & 15u;
    *(u32x4*)(lds + c * CSTR + off * 16 + t * 16) = v;
  }
}

DI void conv_compute(const ConvRegs& R, int d, char* lds, const u16* __restrict__ ssmD,
                     char* __restrict__ ws) {
  const int tid = threadIdx.x, lane = tid & 63, w = tid >> 6;
  const int r = lane & 31, hh = lane >> 5;
  {
    const u32x4 z4 = {0u, 0u, 0u, 0u};
    conv_write_copies(lds, tid, R.h0, R.h1);
    if (tid < 4) conv_write_copies(lds, 256 + tid, R.hx0, R.hx1);
    u32x4 rv;
#pragma unroll
    for (int i = 0; i < 4; ++i) rv[i] = __builtin_amdgcn_alignbit(R.xv[3 - i], R.xv[3 - i], 16);
    *(u32x4*)(lds + XR_OFF + ((tid >> 2) + 31) * XR_STR + (3 - (tid & 3)) * 16) = rv;
    if (tid < 124) *(u32x4*)(lds + XR_OFF + (tid >> 2) * XR_STR + (tid & 3) * 16) = z4;
  }
  __syncthreads();
  if (!(d & 1)) STAMP(1, 1); else STAMP(1, 4);

  f32x16 acc0, acc1a, acc1b;
#pragma unroll
  for (int i = 0; i < 16; ++i) { acc0[i] = 0.f; acc1a[i] = 0.f; acc1b[i] = 0.f; }
  {
    const int c = (1 + r) & 7, u = (1 + r) >> 3;
    const unsigned off = (0x5D9159D0u >> (4 * c)) & 15u;
    const char* abase = lds + c * CSTR + off * 16 + (16 + 8 * hh + 8 * u) * 2 + w * 8 * 64;
    const char* bbase = lds + XR_OFF + (r + 31 - w * 8) * XR_STR + 16 * hh;
#pragma unroll
    for (int gi = 0; gi < 8; ++gi) {
#pragma unroll
      for (int ks = 0; ks < 2; ++ks) {
        const bf16x8 A0 = *(const bf16x8*)(abase + gi * 64 - ks * 32);
        const bf16x8 A1 = *(const bf16x8*)(abase + gi * 64 - ks * 32 + 32 * 64);
        const bf16x8 B0 = *(const bf16x8*)(bbase - gi * XR_STR + (1 - ks) * 32);
        const bf16x8 B1 = *(const bf16x8*)(bbase + (32 - gi) * XR_STR + (1 - ks) * 32);
        acc0 = MFMA(A0, B0, acc0);
        acc1a = MFMA(A1, B0, acc1a);
        acc1b = MFMA(A0, B1, acc1b);
      }
    }
  }
  __syncthreads();
  if (!(d & 1)) STAMP(1, 5); else STAMP(1, 6);
  {
    float* red = (float*)lds;
#pragma unroll
    for (int dst = 0; dst < 4; ++dst)
#pragma unroll
      for (int hf = 0; hf < 2; ++hf) {
        f32x4 v;
#pragma unroll
        for (int i = 0; i < 4; ++i) {
          const int rg = 8 * (dst & 1) + 4 * hf + i;
          v[i] = (dst >> 1) ? (acc1a[rg] + acc1b[rg]) : acc0[rg];
        }
        *(f32x4*)(red + ((((w * 4 + dst) * 2 + hf) * 64) + lane) * 4) = v;
      }
  }
  __syncthreads();
  {
    const float* red = (const float*)lds;
    const float Dv = bf2f(ssmD[d]);
    u16* aT = (u16*)(ws + WS_AT) + (size_t)d * SEQ;
    const int ablk = 32 * (w >> 1) + r;
#pragma unroll
    for (int hf = 0; hf < 2; ++hf) {
      f32x4 y = {0.f, 0.f, 0.f, 0.f};
#pragma unroll
      for (int src = 0; src < 4; ++src) {
        const f32x4 v = *(const f32x4*)(red + ((((src * 4 + w) * 2 + hf) * 64) + lane) * 4);
        y += v;
      }
      const int b0 = 16 * (w & 1) + 8 * hf + 4 * hh;
      const int s = 32 * ablk + b0;
      const u32x2 xv = *(const u32x2*)(lds + XR_OFF + (ablk + 31) * XR_STR + (28 - b0) * 2);
      float kv[4] = {bfhi(xv[1]), bflo(xv[1]), bfhi(xv[0]), bflo(xv[0])};
      float a[4];
#pragma unroll
      for (int i = 0; i < 4; ++i) {
        a[i] = rbf(y[i]) + rbf(kv[i] * Dv);
      }
      u32x2 o;
      o[0] = pk2(a[0], a[1]); o[1] = pk2(a[2], a[3]);
      *(u32x2*)(aT + s) = o;
    }
  }
  __syncthreads();
}

DI void attn_task(const char* lds, int lt0, int qb, bf16x8 qf, int h, char* __restrict__ ws) {
  const int lane = threadIdx.x & 63;
  const int c = lane & 31, hh = lane >> 5;
  f32x16 zero;
#pragma unroll
  for (int i = 0; i < 16; ++i) zero[i] = 0.f;
  f32x16 sc[9];
  const char* kb = lds + lt0 * 1024 + c * 32 + hh * 16;
#pragma unroll
  for (int t = 0; t < 9; ++t) {
    const bf16x8 kf = *(const bf16x8*)(kb + t * 1024);
    sc[t] = MFMA(kf, qf, zero);
  }
  const float NEG = -1e30f;
#pragma unroll
  for (int i = 0; i < 16; ++i) {
    const int row = (i & 3) + 8 * (i >> 2) + 4 * hh;
    sc[0][i] = (row > c) ? sc[0][i] : NEG;
    sc[8][i] = (row <= c) ? sc[8][i] : NEG;
  }
  if (qb < 8) {
#pragma unroll
    for (int t = 0; t < 8; ++t)
      if (qb - 8 + t < 0) {
#pragma unroll
        for (int i = 0; i < 16; ++i) sc[t][i] = NEG;
      }
  }
  float m = NEG;
#pragma unroll
  for (int t = 0; t < 9; ++t)
#pragma unroll
    for (int i = 0; i < 16; ++i) m = fmaxf(m, sc[t][i]);
  m = fmaxf(m, __shfl_xor(m, 32));
  const float cs = 0.25f * 1.4426950408889634f;
  const float mc = m * cs;
  f32x16 oacc[2];
  oacc[0] = zero; oacc[1] = zero;
  const unsigned onesw = (c == 16 || c == 20) ? 0x3F803F80u : 0u;
  const char* vb = lds + 16384 + lt0 * 1024 + (c & 15) * 32 + hh * 16;
#pragma unroll
  for (int t = 0; t < 9; ++t) {
#pragma unroll
    for (int s = 0; s < 2; ++s) {
      u32x4 vw = *(const u32x4*)(vb + t * 1024 + s * 512);
      if (c >= 16) { vw[0] = onesw; vw[1] = onesw; vw[2] = onesw; vw[3] = onesw; }
      u32x4 pw;
#pragma unroll
      for (int e = 0; e < 4; ++e) {
        const float p0 = __builtin_amdgcn_exp2f(fmaf(sc[t][8 * s + 2 * e], cs, -mc));
        const float p1 = __builtin_amdgcn_exp2f(fmaf(sc[t][8 * s + 2 * e + 1], cs, -mc));
        pw[e] = pk2(p0, p1);
      }
      oacc[s] = MFMA(__builtin_bit_cast(bf16x8, vw), __builtin_bit_cast(bf16x8, pw), oacc[s]);
    }
  }
  const float inv = 1.f / (oacc[0][8] + oacc[1][8]);
  unsigned a0 = pk2((oacc[0][0] + oacc[1][0]) * inv, (oacc[0][1] + oacc[1][1]) * inv);
  unsigned a1 = pk2((oacc[0][2] + oacc[1][2]) * inv, (oacc[0][3] + oacc[1][3]) * inv);
  unsigned b0 = pk2((oacc[0][4] + oacc[1][4]) * inv, (oacc[0][5] + oacc[1][5]) * inv);
  unsigned b1 = pk2((oacc[0][6] + oacc[1][6]) * inv, (oacc[0][7] + oacc[1][7]) * inv);
  auto r0 = __builtin_amdgcn_permlane32_swap(a0, b0, false, false);
  auto r1 = __builtin_amdgcn_permlane32_swap(a1, b1, false, false);
  u32x4 wv = {r0[0], r1[0], r0[1], r1[1]};
  u16* o = (u16*)(ws + WS_O) + ((size_t)((qb * 32 + h) * 2 + hh) * 32 + c) * 8;
  *(u32x4*)o = wv;
}

DI void attn_body(int h, int g, char* lds, char* __restrict__ ws) {
  const int tid = threadIdx.x, lane = tid & 63;
  const int w = __builtin_amdgcn_readfirstlane(tid >> 6);
  const int c = lane & 31, hh = lane >> 5;
  const u16* Qh = (const u16*)(ws + WS_Q) + (size_t)h * SEQ * DH;
  const u16* Kh = (const u16*)(ws + WS_K) + (size_t)h * SEQ * DH;
  const u16* Vt = (const u16*)(ws + WS_VT) + (size_t)h * 64 * 512;
#pragma unroll
  for (int i = 0; i < 4; ++i) {
    const int lt = w * 4 + i;
    int gt = 8 * g - 8 + lt; gt = gt < 0 ? 0 : gt;
    __builtin_amdgcn_global_load_lds((GLB_AS const void*)(Kh + (size_t)gt * 512 + lane * 8),
                                     (LDS_AS void*)(lds + lt * 1024), 16, 0, 0);
    __builtin_amdgcn_global_load_lds((GLB_AS const void*)(Vt + (size_t)gt * 512 + lane * 8),
                                     (LDS_AS void*)(lds + 16384 + lt * 1024), 16, 0, 0);
  }
  const int qb0 = 8 * g + w, qb1 = 8 * g + w + 4;
  const bf16x8 q0 = *(const bf16x8*)(Qh + (size_t)(32 * qb0 + c) * DH + 8 * hh);
  const bf16x8 q1 = *(const bf16x8*)(Qh + (size_t)(32 * qb1 + c) * DH + 8 * hh);
  asm volatile("s_waitcnt vmcnt(0)" ::: "memory");
  __syncthreads();
  STAMP(1, 1);
  attn_task(lds, w, qb0, q0, h, ws);
  STAMP(1, 2);
  attn_task(lds, w + 4, qb1, q1, h, ws);
}

__global__ __launch_bounds__(256, 2) void k2_attn_conv(const u16* __restrict__ ssmk,
                                                       const u16* __restrict__ ssmD,
                                                       char* __restrict__ ws) {
  __shared__ __attribute__((aligned(16))) char lds[K2_LDS];
  const int b = blockIdx.x;
  STAMP(1, 0);
  if (b >= 256) {
    const int a = b - 256;
    attn_body(a >> 3, a & 7, lds, ws);
  } else {
    ConvRegs R0, R1;
    conv_load(R0, 2 * b, ssmk, ws);
    conv_load(R1, 2 * b + 1, ssmk, ws);
    conv_compute(R0, 2 * b, lds, ssmD, ws);
    STAMP(1, 2);
    conv_compute(R1, 2 * b + 1, lds, ssmD, ws);
  }
  STAMP(1, 3);
}

__global__ __launch_bounds__(256, 2) void k3_outproj(char* __restrict__ ws, const u16* __restrict__ Aq, u16* __restrict__ out) {
  const int tid = threadIdx.x, lane = tid & 63, w = tid >> 6;
  const int c = lane & 31, hh = lane >> 5;
  const int b = blockIdx.x;
  STAMP(2, 0);
  const int st = b >> 3, ntl = b & 7;
  const int ntile = ntl * 2 + (w & 1), stile = st * 2 + (w >> 1);
  const int n0 = ntile * 32, s0 = stile * 32;
  const u16* woT = (const u16*)(ws + WS_WOT) + (size_t)ntile * 32 * 512 + lane * 8;
  const u16* o = (const u16*)(ws + WS_O) + (size_t)stile * 32 * 512 + lane * 8;
  const int s = s0 + c;
  const float g = ((const float*)(ws + WS_G))[s];
  const u16* aT = (const u16*)(ws + WS_AT);
  unsigned av[16];
#pragma unroll
  for (int i = 0; i < 16; ++i) av[i] = aT[(size_t)(n0 + (i & 3) + 8 * (i >> 2) + 4 * hh) * SEQ + s];
  u32x2 qv[4];
#pragma unroll
  for (int gq = 0; gq < 4; ++gq) qv[gq] = *(const u32x2*)(Aq + (size_t)s * DM + n0 + 8 * gq + 4 * hh);
  f32x16 acc;
#pragma unroll
  for (int i = 0; i < 16; ++i) acc[i] = 0.f;
#pragma unroll
  for (int ks = 0; ks < 32; ++ks) {
    const bf16x8 af = *(const bf16x8*)(woT + 512 * ks);
    const bf16x8 bfr = *(const bf16x8*)(o + 512 * ks);
    acc = MFMA(af, bfr, acc);
  }
  STAMP(2, 2);
  const float omg = rbf(1.f - g);
#pragma unroll
  for (int gq = 0; gq < 4; ++gq) {
    const int n = n0 + 8 * gq + 4 * hh;
    float res[4];
#pragma unroll
    for (int i = 0; i < 4; ++i) {
      const float qf = (i & 1) ? bfhi(qv[gq][i >> 1]) : bflo(qv[gq][i >> 1]);
      const float a = rbf(qf * bf2f(av[4 * gq + i]));
      const float bb = rbf(acc[4 * gq + i]);
      res[i] = rbf(g * a) + rbf(omg * bb);
    }
    u32x2 wv;
    wv[0] = pk2(res[0], res[1]); wv[1] = pk2(res[2], res[3]);
    *(u32x2*)(out + (size_t)s * DM + n) = wv;
  }
  STAMP(2, 3);
}

extern "C" void kernel_launch(void* const* d_in, const int* in_sizes, int n_in,
                              void* d_out, int out_size, void* d_ws, size_t ws_size,
                              hipStream_t stream) {
  const u16* Aq = (const u16*)d_in[0];
  const u16* Ak = (const u16*)d_in[1];
  const u16* Av = (const u16*)d_in[2];
  const u16* shiftk = (const u16*)d_in[3];
  const u16* ssmk = (const u16*)d_in[4];
  const u16* ssmD = (const u16*)d_in[5];
  const u16* wqkv = (const u16*)d_in[7];
  const u16* wo = (const u16*)d_in[8];
  const u16* gatew = (const u16*)d_in[9];
  char* ws = (char*)d_ws;
  k1_gemm_prep<<<256, 512, 0, stream>>>(Aq, Ak, Av, shiftk, wqkv, wo, gatew, ws);
  k2_attn_conv<<<512, 256, 0, stream>>>(ssmk, ssmD, ws);
  k3_outproj<<<256, 256, 0, stream>>>(ws, Aq, (u16*)d_out);
}
```

```cpp
#include <hip/hip_runtime.h>
#include <hip/hip_bf16.h>
#include <stdint.h>

#define SEQ 2048
#define DM 512
#define NH 32
#define DH 16

typedef __attribute__((ext_vector_type(8))) short bf16x8;
typedef __attribute__((ext_vector_type(4))) short s16x4;
typedef __attribute__((ext_vector_type(16))) float f32x16;
typedef __attribute__((ext_vector_type(4))) float f32x4;
typedef __attribute__((ext_vector_type(4))) unsigned u32x4;
typedef __attribute__((ext_vector_type(2))) unsigned u32x2;
typedef __attribute__((ext_vector_type(2))) __bf16 bf16x2_t;
typedef unsigned short u16;

#define DI __device__ __forceinline__
#define MFMA(a, b, c) __builtin_amdgcn_mfma_f32_32x32x16_bf16((a), (b), (c), 0, 0, 0)
#define LDS_AS __attribute__((address_space(3)))
#define GLB_AS __attribute__((address_space(1)))

DI float bf2f(unsigned v16) { return __uint_as_float(v16 << 16); }
DI float bflo(unsigned w) { return __uint_as_float(w << 16); }
DI float bfhi(unsigned w) { return __uint_as_float(w & 0xffff0000u); }
DI unsigned pk2(float lo, float hi) {
  bf16x2_t v; v[0] = (__bf16)lo; v[1] = (__bf16)hi;
  return __builtin_bit_cast(unsigned, v);
}
DI float rbf(float x) {
  bf16x2_t v; v[0] = (__bf16)x; v[1] = (__bf16)0.f;
  return __uint_as_float(__builtin_bit_cast(unsigned, v) << 16);
}

#define STAMP(kid, slot)
#define WS_Q   (0u)
#define WS_K   (2u << 20)
#define WS_VT  (4u << 20)
#define WS_KVT (6u << 20)
#define WS_QT  (8u << 20)
#define WS_AT  (10u << 20)
#define WS_O   (12u << 20)
#define WS_WOT (14u << 20)
#define WS_G   (15u << 20)

#define K1_WBYTES (512 * 192)
#define K1_XW 16384

DI void glds16(unsigned voff, const void* sbase, unsigned ldsaddr) {
  asm volatile("s_mov_b32 m0, %2\n\ts_nop 0\n\tglobal_load_lds_dwordx4 %0, %1"
               :: "v"(voff), "s"(sbase), "s"(ldsaddr) : "memory");
}
DI void k1_issue_w(const u16* __restrict__ W, unsigned ldsb, int half, int n0, int w, const unsigned (&wo3)[3], int rot) {
#pragma unroll
  for (int i = 0; i < 4; ++i) {
    const int g = half * 16 + ((4 * i + w + rot) & 15);
    const char* sb = (const char*)W + (size_t)g * (16 * 3072) + n0 * 2;
#pragma unroll
    for (int j = 0; j < 3; ++j) glds16(wo3[j], sb, ldsb + g * 3072 + j * 1024);
  }
}
DI void k1_issue_x(const u16* __restrict__ X, unsigned xb, int half, int m0, unsigned xo) {
#pragma unroll
  for (int i = 0; i < 16; ++i) {
    const char* sb = (const char*)X + (size_t)(m0 + 2 * i) * 1024 + half * 512;
    glds16(xo ^ (unsigned)(((2 * i) & 15) << 4), sb, xb + i * 1024);
  }
}

template <int OFF>
DI void tr_rd(s16x4& d, unsigned base) {
  asm volatile("ds_read_b64_tr_b16 %0, %1 offset:%2" : "=v"(d) : "v"(base), "i"(OFF));
}
template <int KS>
DI void k1_rdb(s16x4 (&t)[6], unsigned base) {
  tr_rd<KS * 3072 + 0>(t[0], base);   tr_rd<KS * 3072 + 768>(t[1], base);
  tr_rd<KS * 3072 + 64>(t[2], base);  tr_rd<KS * 3072 + 64 + 768>(t[3], base);
  tr_rd<KS * 3072 + 128>(t[4], base); tr_rd<KS * 3072 + 128 + 768>(t[5], base);
}
template <int N>
DI void lgkm_wait6(s16x4 (&t)[6]) {
  asm volatile("s_waitcnt lgkmcnt(%6)" : "+v"(t[0]), "+v"(t[1]), "+v"(t[2]), "+v"(t[3]), "+v"(t[4]), "+v"(t[5]) : "i"(N));
}
template <int NV, int KS>
DI void k1_steps(const bf16x8 (&af)[16], unsigned wb, f32x16 (&acc)[3], s16x4 (&t0)[6], s16x4 (&t1)[6], s16x4 (&t2)[6]) {
  if constexpr (KS + 1 < 16) lgkm_wait6<6>(t0); else lgkm_wait6<0>(t0);
#pragma unroll
  for (int j = 0; j < 3; ++j) {
    const bf16x8 bfr = __builtin_shufflevector(t0[2 * j], t0[2 * j + 1], 0, 1, 2, 3, 4, 5, 6, 7);
    if (j >= 3 - NV) acc[j] = MFMA(af[KS], bfr, acc[j]);
    else             acc[j] = MFMA(bfr, af[KS], acc[j]);
    if (j == 0) { if constexpr (KS + 2 < 16) k1_rdb<KS + 2>(t2, wb); }
  }
  __builtin_amdgcn_sched_barrier(0);
  if constexpr (KS + 1 < 16) k1_steps<NV, KS + 1>(af, wb, acc, t1, t2, t0);
}
template <int NV>
DI void k1_half(const bf16x8 (&af)[16], unsigned wb, f32x16 (&acc)[3]) {
  s16x4 t0[6], t1[6], t2[6];
  k1_rdb<0>(t0, wb);
  k1_rdb<1>(t1, wb);
  k1_steps<NV, 0>(af, wb, acc, t0, t1, t2);
}

struct PrepIn { unsigned kk[11], vv[8], wv[4]; u32x4 skw, gw, gx[2]; };
struct PrepOut { u32x4 kv[2]; u32x2 wo; float gv[2]; };
DI void prep_store(const PrepOut& O, char* __restrict__ ws, int b, int pw, int lane);

template <int NV>
DI void gemm_body(const u16* __restrict__ X, char* lds, int mbase, int n0,
                  int w, int lane, u16* __restrict__ Qh, u16* __restrict__ Kh,
                  u16* __restrict__ Vt, char* ws, const PrepOut& O, int b) {
  const int r = lane & 31, hh = lane >> 5;
  f32x16 acc[3];
#pragma unroll
  for (int j = 0; j < 3; ++j)
#pragma unroll
    for (int i = 0; i < 16; ++i) acc[j][i] = 0.f;

  const int g = (lane >> 4) & 1, q = (lane & 15) >> 2, p = lane & 3;
  const unsigned ldsb = (unsigned)(size_t)lds;
  const unsigned wb = ldsb + (8 * hh + q) * 192 + (16 * g + 4 * p) * 2;
  const unsigned xb = ldsb + K1_WBYTES + w * K1_XW;
  const unsigned xo = (unsigned)(hh * 1024 + ((r ^ hh) << 4));
  const unsigned xa = ldsb + K1_WBYTES + w * K1_XW + r * 512;
  const int m0 = mbase + 32 * w;

  bf16x8 af[16];
  asm volatile("s_waitcnt vmcnt(12)" ::: "memory");
  __builtin_amdgcn_s_barrier();
  asm volatile("" ::: "memory");
  STAMP(0, 1);
#pragma unroll
  for (int ks = 0; ks < 16; ++ks) af[ks] = *(const LDS_AS bf16x8*)(xa + (((2 * ks + hh) ^ (r & 15)) * 16));
  asm volatile("s_waitcnt lgkmcnt(0)" ::: "memory");
  k1_issue_x(X, xb, 1, m0, xo);
  prep_store(O, ws, b, w, lane);
  asm volatile("" ::: "memory");
  k1_half<NV>(af, wb, acc);
  asm volatile("s_waitcnt vmcnt(0)" ::: "memory");
  __builtin_amdgcn_s_barrier();
  asm volatile("" ::: "memory");
  STAMP(0, 4);
#pragma unroll
  for (int ks = 0; ks < 16; ++ks) af[ks] = *(const LDS_AS bf16x8*)(xa + (((2 * ks + hh) ^ (r & 15)) * 16));
  asm volatile("s_waitcnt lgkmcnt(0)" ::: "memory");
  k1_half<NV>(af, wb + 256 * 192, acc);
  STAMP(0, 2);
#pragma unroll
  for (int j = 0; j < 3; ++j) {
    const int nc = n0 + 32 * j;
    if (j >= 3 - NV) {
      const int nv = nc - 1024 + r;
      const int head = nv >> 4, d = nv & 15;
      u16* dst = Vt + (size_t)(head * 64 + (m0 >> 5)) * 512 + d * 16 + 8 * hh;
      u32x4 w0, w1;
      w0[0] = pk2(acc[j][0], acc[j][1]);  w0[1] = pk2(acc[j][2], acc[j][3]);
      w0[2] = pk2(acc[j][4], acc[j][5]);  w0[3] = pk2(acc[j][6], acc[j][7]);
      w1[0] = pk2(acc[j][8], acc[j][9]);  w1[1] = pk2(acc[j][10], acc[j][11]);
      w1[2] = pk2(acc[j][12], acc[j][13]); w1[3] = pk2(acc[j][14], acc[j][15]);
      *(u32x4*)(dst) = w0;
      *(u32x4*)(dst + 256) = w1;
    } else {
      u16* base = (nc < 512) ? Qh : Kh;
      const int head0 = (nc & 511) >> 4;
#pragma unroll
      for (int e = 0; e < 2; ++e) {
        unsigned a0 = pk2(acc[j][8 * e], acc[j][8 * e + 1]), a1 = pk2(acc[j][8 * e + 2], acc[j][8 * e + 3]);
        unsigned b0 = pk2(acc[j][8 * e + 4], acc[j][8 * e + 5]), b1 = pk2(acc[j][8 * e + 6], acc[j][8 * e + 7]);
        auto r0 = __builtin_amdgcn_permlane32_swap(a0, b0, false, false);
        auto r1 = __builtin_amdgcn_permlane32_swap(a1, b1, false, false);
        u32x4 wv = {r0[0], r1[0], r0[1], r1[1]};
        u16* dst = base + ((size_t)(head0 + e) * SEQ + m0 + r) * DH + 8 * hh;
        *(u32x4*)dst = wv;
      }
    }
  }
}

#define GLD1(dst, voff, sb, imm) asm volatile("global_load_dword %0, %1, %2 offset:" #imm : "=v"(dst) : "v"(voff), "s"(sb))
#define GLD4(dst, voff, sb, imm) asm volatile("global_load_dwordx4 %0, %1, %2 offset:" #imm : "=v"(dst) : "v"(voff), "s"(sb))
#define GLDH(dst, voff, sb, imm) asm volatile("global_load_ushort %0, %1, %2 offset:" #imm : "=v"(dst) : "v"(voff), "s"(sb))
DI void prep_load(PrepIn& P, const u16* __restrict__ Aq, const u16* __restrict__ Ak, const u16* __restrict__ Av,
                  const u16* __restrict__ shiftk, const u16* __restrict__ wo, const u16* __restrict__ gatew,
                  int b, int pw, int lane) {
  const int st = b >> 3, dt = b & 7;
  const int dg = lane & 7, sg = lane >> 3;
  const int d0 = dt * 64 + dg * 8 + 2 * pw;
  const int s0 = st * 64 + sg * 8;
  const unsigned va = (unsigned)((s0 == 0 ? 0 : s0 - 3) * 1024 + d0 * 2);
  const unsigned vb = (unsigned)(s0 * 1024 + d0 * 2);
  const char* k2 = (const char*)Ak + 4096;
  const char* v2 = (const char*)Av + 4096;
  GLD1(P.kk[0], va, Ak, 0); GLD1(P.kk[1], va, Ak, 1024); GLD1(P.kk[2], va, Ak, 2048);
  GLD1(P.kk[3], vb, Ak, 0); GLD1(P.kk[4], vb, Ak, 1024); GLD1(P.kk[5], vb, Ak, 2048); GLD1(P.kk[6], vb, Ak, 3072);
  GLD1(P.kk[7], vb, k2, 0); GLD1(P.kk[8], vb, k2, 1024); GLD1(P.kk[9], vb, k2, 2048); GLD1(P.kk[10], vb, k2, 3072);
  GLD1(P.vv[0], vb, Av, 0); GLD1(P.vv[1], vb, Av, 1024); GLD1(P.vv[2], vb, Av, 2048); GLD1(P.vv[3], vb, Av, 3072);
  GLD1(P.vv[4], vb, v2, 0); GLD1(P.vv[5], vb, v2, 1024); GLD1(P.vv[6], vb, v2, 2048); GLD1(P.vv[7], vb, v2, 3072);
  const unsigned vs = (unsigned)(d0 * 8), vl = (unsigned)(lane * 16), vh = (unsigned)(lane * 2);
  GLD4(P.skw, vs, shiftk, 0);
  GLD4(P.gw, vl, gatew, 0);
  const char* gq = (const char*)Aq + (size_t)(b * 8 + pw * 2) * 1024;
  GLD4(P.gx[0], vl, gq, 0); GLD4(P.gx[1], vl, gq, 1024);
  const char* wr = (const char*)wo + (size_t)((b >> 3) * 16 + pw * 4) * 1024 + (b & 7) * 128;
  GLDH(P.wv[0], vh, wr, 0); GLDH(P.wv[1], vh, wr, 1024); GLDH(P.wv[2], vh, wr, 2048); GLDH(P.wv[3], vh, wr, 3072);
}
DI void prep_wait(PrepIn& P) {
  asm volatile("s_waitcnt vmcnt(40)"
               : "+v"(P.kk[0]), "+v"(P.kk[1]), "+v"(P.kk[2]), "+v"(P.kk[3]), "+v"(P.kk[4]), "+v"(P.kk[5]),
                 "+v"(P.kk[6]), "+v"(P.kk[7]), "+v"(P.kk[8]), "+v"(P.kk[9]), "+v"(P.kk[10]),
                 "+v"(P.vv[0]), "+v"(P.vv[1]), "+v"(P.vv[2]), "+v"(P.vv[3]), "+v"(P.vv[4]), "+v"(P.vv[5]),
                 "+v"(P.vv[6]), "+v"(P.vv[7]));
  asm volatile("" : "+v"(P.skw), "+v"(P.gw), "+v"(P.gx[0]), "+v"(P.gx[1]),
                    "+v"(P.wv[0]), "+v"(P.wv[1]), "+v"(P.wv[2]), "+v"(P.wv[3]));
}

DI void prep_compute(PrepIn& P, PrepOut& O, int b, int lane) {
  const int s0 = (b >> 3) * 64 + (lane >> 3) * 8;
  if (s0 == 0) { P.kk[0] = 0; P.kk[1] = 0; P.kk[2] = 0; }
#pragma unroll
  for (int c = 0; c < 2; ++c) {
    const unsigned s01 = P.skw[c * 2], s23 = P.skw[c * 2 + 1];
    const float sk0 = bflo(s01), sk1 = bfhi(s01), sk2 = bflo(s23), sk3 = bfhi(s23);
    float kf[11];
#pragma unroll
    for (int i = 0; i < 11; ++i) kf[i] = c ? bfhi(P.kk[i]) : bflo(P.kk[i]);
    float kvv[8];
#pragma unroll
    for (int i = 0; i < 8; ++i) {
      float acc = rbf(kf[i] * sk3);
      acc = rbf(acc + rbf(kf[i + 1] * sk2));
      acc = rbf(acc + rbf(kf[i + 2] * sk1));
      acc = rbf(acc + rbf(kf[i + 3] * sk0));
      const float vf = c ? bfhi(P.vv[i]) : bflo(P.vv[i]);
      kvv[i] = acc * vf;
    }
    O.kv[c][0] = pk2(kvv[0], kvv[1]); O.kv[c][1] = pk2(kvv[2], kvv[3]);
    O.kv[c][2] = pk2(kvv[4], kvv[5]); O.kv[c][3] = pk2(kvv[6], kvv[7]);
  }
  O.wo[0] = P.wv[0] | (P.wv[1] << 16); O.wo[1] = P.wv[2] | (P.wv[3] << 16);
#pragma unroll
  for (int i = 0; i < 2; ++i) {
    float sum = 0.f;
#pragma unroll
    for (int e = 0; e < 4; ++e) {
      sum += rbf(bflo(P.gx[i][e]) * bflo(P.gw[e]));
      sum += rbf(bfhi(P.gx[i][e]) * bfhi(P.gw[e]));
    }
#pragma unroll
    for (int o = 32; o > 0; o >>= 1) sum += __shfl_xor(sum, o);
    const float z = rbf(sum);
    O.gv[i] = rbf(1.f / (1.f + __expf(-z)));
  }
}

DI void prep_store(const PrepOut& O, char* __restrict__ ws, int b, int pw, int lane) {
  const int st = b >> 3, dt = b & 7;
  const int dg = lane & 7, sg = lane >> 3;
  const int d0 = dt * 64 + dg * 8 + 2 * pw;
  const int s0 = st * 64 + sg * 8;
  u16* kvT = (u16*)(ws + WS_KVT);
  *(u32x4*)(kvT + (size_t)d0 * SEQ + s0) = O.kv[0];
  *(u32x4*)(kvT + (size_t)(d0 + 1) * SEQ + s0) = O.kv[1];
  const int wk0 = (b >> 3) * 16 + pw * 4, wn = (b & 7) * 64 + lane;
  u16* woT = (u16*)(ws + WS_WOT);
  const size_t off = ((size_t)(((wn >> 5) * 32 + (wk0 >> 4)) * 2 + ((wk0 >> 3) & 1)) * 32 + (wn & 31)) * 8 + (wk0 & 7);
  *(u32x2*)(woT + off) = O.wo;
  float* gout = (float*)(ws + WS_G);
  if (lane < 2) gout[b * 8 + pw * 2 + lane] = lane ? O.gv[1] : O.gv[0];
}

__global__ __launch_bounds__(256, 1) void k1_gemm_prep(
    const u16* __restrict__ Aq, const u16* __restrict__ Ak, const u16* __restrict__ Av,
    const u16* __restrict__ shiftk, const u16* __restrict__ wqkv, const u16* __restrict__ wo,
    const u16* __restrict__ gatew, char* __restrict__ ws) {
  __shared__ __attribute__((aligned(16))) char lds[K1_WBYTES + 4 * K1_XW];
  const int tid = threadIdx.x, lane = tid & 63;
  const int w = __builtin_amdgcn_readfirstlane(tid >> 6);
  const int b = blockIdx.x;
  STAMP(0, 0);
  const int xcd = b & 7, idx = b >> 3;
  const int mt = (xcd >> 1) * 4 + (idx >> 3);
  const int nt = (xcd & 1) * 8 + (idx & 7);
  const int n0 = nt * 96, mbase = mt * 128;
  const int rot = (idx >> 3) * 4;
  const unsigned ldsb = (unsigned)(size_t)lds;
  unsigned wo3[3];
#pragma unroll
  for (int j = 0; j < 3; ++j) {
    const int ci = j * 64 + lane, row = ci / 12, c16 = ci - row * 12;
    wo3[j] = (unsigned)(row * 3072 + c16 * 16);
  }
  const unsigned xo = (unsigned)((lane >> 5) * 1024 + (((lane & 31) ^ (lane >> 5)) << 4));

  PrepIn P;
  prep_load(P, Aq, Ak, Av, shiftk, wo, gatew, b, w, lane);
  k1_issue_w(wqkv, ldsb, 0, n0, w, wo3, rot);
  k1_issue_x(Aq, ldsb + K1_WBYTES + w * K1_XW, 0, mbase + 32 * w, xo);
  k1_issue_w(wqkv, ldsb, 1, n0, w, wo3, rot);
  prep_wait(P);
  PrepOut O;
  prep_compute(P, O, b, lane);
  __builtin_amdgcn_sched_barrier(0);

  u16* Qh = (u16*)(ws + WS_Q);
  u16* Kh = (u16*)(ws + WS_K);
  u16* Vt = (u16*)(ws + WS_VT);
  if (nt <= 9) gemm_body<0>(Aq, lds, mbase, n0, w, lane, Qh, Kh, Vt, ws, O, b);
  else if (nt == 10) gemm_body<1>(Aq, lds, mbase, n0, w, lane, Qh, Kh, Vt, ws, O, b);
  else gemm_body<3>(Aq, lds, mbase, n0, w, lane, Qh, Kh, Vt, ws, O, b);
  STAMP(0, 3);
}

#define CSTR 4608
#define XR_OFF (8 * CSTR)
#define XR_STR 80
#define K2_LDS (XR_OFF + 95 * XR_STR)

struct ConvRegs { u32x4 h0, h1, hx0, hx1, xv; };

DI void conv_load(ConvRegs& R, int d, const u16* __restrict__ ssmk, char* __restrict__ ws) {
  const int tid = threadIdx.x, lane = tid & 63, w = tid >> 6;
  const int r = lane & 31, hh = lane >> 5;
  const u16* hrow = ssmk + (size_t)d * SEQ;
  const u32x4 z4 = {0u, 0u, 0u, 0u};
  R.h0 = *(const u32x4*)(hrow + 8 * (tid >= 4 ? tid - 4 : 0));
  R.h1 = *(const u32x4*)(hrow + 8 * (tid >= 3 ? tid - 3 : 0));
  if (tid < 4) R.h0 = z4;
  if (tid < 3) R.h1 = z4;
  R.hx0 = z4; R.hx1 = z4;
  if (tid < 4) {
    R.hx0 = *(const u32x4*)(hrow + 8 * (252 + tid));
    if (tid < 3) R.hx1 = *(const u32x4*)(hrow + 8 * (253 + tid));
  }
  R.xv = *(const u32x4*)((const u16*)(ws + WS_KVT) + (size_t)d * SEQ + 8 * tid);
}

DI void conv_write_copies(char* lds, int t, u32x4 h0, u32x4 h1) {
  unsigned wv[8] = {h0[0], h0[1], h0[2], h0[3], h1[0], h1[1], h1[2], h1[3]};
  unsigned od[7];
#pragma unroll
  for (int i = 0; i < 7; ++i) od[i] = __builtin_amdgcn_alignbyte(wv[i + 1], wv[i], 2);
#pragma unroll
  for (int c = 0; c < 8; ++c) {
    const int e = c >> 1;
    u32x4 v;
    if (c & 1) { v[0] = od[e]; v[1] = od[e + 1]; v[2] = od[e + 2]; v[3] = od[e + 3]; }
    else       { v[0] = wv[e]; v[1] = wv[e + 1]; v[2] = wv[e + 2]; v[3] = wv[e + 3]; }
    const unsigned off = (0x5D9159D0u >> (4 * c)) & 15u;
    *(u32x4*)(lds + c * CSTR + off * 16 + t * 16) = v;
  }
}

DI void conv_compute(const ConvRegs& R, int d, char* lds, const u16* __restrict__ ssmD,
                     char* __restrict__ ws) {
  const int tid = threadIdx.x, lane = tid & 63, w = tid >> 6;
  const int r = lane & 31, hh = lane >> 5;
  {
    const u32x4 z4 = {0u, 0u, 0u, 0u};
    conv_write_copies(lds, tid, R.h0, R.h1);
    if (tid < 4) conv_write_copies(lds, 256 + tid, R.hx0, R.hx1);
    u32x4 rv;
#pragma unroll
    for (int i = 0; i < 4; ++i) rv[i] = __builtin_amdgcn_alignbit(R.xv[3 - i], R.xv[3 - i], 16);
    *(u32x4*)(lds + XR_OFF + ((tid >> 2) + 31) * XR_STR + (3 - (tid & 3)) * 16) = rv;
    if (tid < 124) *(u32x4*)(lds + XR_OFF + (tid >> 2) * XR_STR + (tid & 3) * 16) = z4;
  }
  __syncthreads();
  if (!(d & 1)) STAMP(1, 1); else STAMP(1, 4);

  f32x16 acc0, acc1a, acc1b;
#pragma unroll
  for (int i = 0; i < 16; ++i) { acc0[i] = 0.f; acc1a[i] = 0.f; acc1b[i] = 0.f; }
  {
    const int c = (1 + r) & 7, u = (1 + r) >> 3;
    const unsigned off = (0x5D9159D0u >> (4 * c)) & 15u;
    const char* abase = lds + c * CSTR + off * 16 + (16 + 8 * hh + 8 * u) * 2 + w * 8 * 64;
    const char* bbase = lds + XR_OFF + (r + 31 - w * 8) * XR_STR + 16 * hh;
#pragma unroll
    for (int gi = 0; gi < 8; ++gi) {
#pragma unroll
      for (int ks = 0; ks < 2; ++ks) {
        const bf16x8 A0 = *(const bf16x8*)(abase + gi * 64 - ks * 32);
        const bf16x8 A1 = *(const bf16x8*)(abase + gi * 64 - ks * 32 + 32 * 64);
        const bf16x8 B0 = *(const bf16x8*)(bbase - gi * XR_STR + (1 - ks) * 32);
        const bf16x8 B1 = *(const bf16x8*)(bbase + (32 - gi) * XR_STR + (1 - ks) * 32);
        acc0 = MFMA(A0, B0, acc0);
        acc1a = MFMA(A1, B0, acc1a);
        acc1b = MFMA(A0, B1, acc1b);
      }
    }
  }
  __syncthreads();
  if (!(d & 1)) STAMP(1, 5); else STAMP(1, 6);
  {
    float* red = (float*)lds;
#pragma unroll
    for (int dst = 0; dst < 4; ++dst)
#pragma unroll
      for (int hf = 0; hf < 2; ++hf) {
        f32x4 v;
#pragma unroll
        for (int i = 0; i < 4; ++i) {
          const int rg = 8 * (dst & 1) + 4 * hf + i;
          v[i] = (dst >> 1) ? (acc1a[rg] + acc1b[rg]) : acc0[rg];
        }
        *(f32x4*)(red + ((((w * 4 + dst) * 2 + hf) * 64) + lane) * 4) = v;
      }
  }
  __syncthreads();
  {
    const float* red = (const float*)lds;
    const float Dv = bf2f(ssmD[d]);
    u16* aT = (u16*)(ws + WS_AT) + (size_t)d * SEQ;
    const int ablk = 32 * (w >> 1) + r;
#pragma unroll
    for (int hf = 0; hf < 2; ++hf) {
      f32x4 y = {0.f, 0.f, 0.f, 0.f};
#pragma unroll
      for (int src = 0; src < 4; ++src) {
        const f32x4 v = *(const f32x4*)(red + ((((src * 4 + w) * 2 + hf) * 64) + lane) * 4);
        y += v;
      }
      const int b0 = 16 * (w & 1) + 8 * hf + 4 * hh;
      const int s = 32 * ablk + b0;
      const u32x2 xv = *(const u32x2*)(lds + XR_OFF + (ablk + 31) * XR_STR + (28 - b0) * 2);
      float kv[4] = {bfhi(xv[1]), bflo(xv[1]), bfhi(xv[0]), bflo(xv[0])};
      float a[4];
#pragma unroll
      for (int i = 0; i < 4; ++i) {
        a[i] = rbf(y[i]) + rbf(kv[i] * Dv);
      }
      u32x2 o;
      o[0] = pk2(a[0], a[1]); o[1] = pk2(a[2], a[3]);
      *(u32x2*)(aT + s) = o;
    }
  }
  __syncthreads();
}

DI void attn_task(const char* lds, int lt0, int qb, bf16x8 qf, int h, char* __restrict__ ws) {
  const int lane = threadIdx.x & 63;
  const int c = lane & 31, hh = lane >> 5;
  f32x16 zero;
#pragma unroll
  for (int i = 0; i < 16; ++i) zero[i] = 0.f;
  f32x16 sc[9];
  const char* kb = lds + lt0 * 1024 + c * 32 + hh * 16;
#pragma unroll
  for (int t = 0; t < 9; ++t) {
    const bf16x8 kf = *(const bf16x8*)(kb + t * 1024);
    sc[t] = MFMA(kf, qf, zero);
  }
  const float NEG = -1e30f;
#pragma unroll
  for (int i = 0; i < 16; ++i) {
    const int row = (i & 3) + 8 * (i >> 2) + 4 * hh;
    sc[0][i] = (row > c) ? sc[0][i] : NEG;
    sc[8][i] = (row <= c) ? sc[8][i] : NEG;
  }
  if (qb < 8) {
#pragma unroll
    for (int t = 0; t < 8; ++t)
      if (qb - 8 + t < 0) {
#pragma unroll
        for (int i = 0; i < 16; ++i) sc[t][i] = NEG;
      }
  }
  float m = NEG;
#pragma unroll
  for (int t = 0; t < 9; ++t)
#pragma unroll
    for (int i = 0; i < 16; ++i) m = fmaxf(m, sc[t][i]);
  m = fmaxf(m, __shfl_xor(m, 32));
  const float cs = 0.25f * 1.4426950408889634f;
  const float mc = m * cs;
  f32x16 oacc[2];
  oacc[0] = zero; oacc[1] = zero;
  const unsigned onesw = (c == 16 || c == 20) ? 0x3F803F80u : 0u;
  const char* vb = lds + 16384 + lt0 * 1024 + (c & 15) * 32 + hh * 16;
#pragma unroll
  for (int t = 0; t < 9; ++t) {
#pragma unroll
    for (int s = 0; s < 2; ++s) {
      u32x4 vw = *(const u32x4*)(vb + t * 1024 + s * 512);
      if (c >= 16) { vw[0] = onesw; vw[1] = onesw; vw[2] = onesw; vw[3] = onesw; }
      u32x4 pw;
#pragma unroll
      for (int e = 0; e < 4; ++e) {
        const float p0 = __builtin_amdgcn_exp2f(fmaf(sc[t][8 * s + 2 * e], cs, -mc));
        const float p1 = __builtin_amdgcn_exp2f(fmaf(sc[t][8 * s + 2 * e + 1], cs, -mc));
        pw[e] = pk2(p0, p1);
      }
      oacc[s] = MFMA(__builtin_bit_cast(bf16x8, vw), __builtin_bit_cast(bf16x8, pw), oacc[s]);
    }
  }
  const float inv = 1.f / (oacc[0][8] + oacc[1][8]);
  unsigned a0 = pk2((oacc[0][0] + oacc[1][0]) * inv, (oacc[0][1] + oacc[1][1]) * inv);
  unsigned a1 = pk2((oacc[0][2] + oacc[1][2]) * inv, (oacc[0][3] + oacc[1][3]) * inv);
  unsigned b0 = pk2((oacc[0][4] + oacc[1][4]) * inv, (oacc[0][5] + oacc[1][5]) * inv);
  unsigned b1 = pk2((oacc[0][6] + oacc[1][6]) * inv, (oacc[0][7] + oacc[1][7]) * inv);
  auto r0 = __builtin_amdgcn_permlane32_swap(a0, b0, false, false);
  auto r1 = __builtin_amdgcn_permlane32_swap(a1, b1, false, false);
  u32x4 wv = {r0[0], r1[0], r0[1], r1[1]};
  u16* o = (u16*)(ws + WS_O) + ((size_t)((qb * 32 + h) * 2 + hh) * 32 + c) * 8;
  *(u32x4*)o = wv;
}

DI void attn_body(int h, int g, char* lds, char* __restrict__ ws) {
  const int tid = threadIdx.x, lane = tid & 63;
  const int w = __builtin_amdgcn_readfirstlane(tid >> 6);
  const int c = lane & 31, hh = lane >> 5;
  const u16* Qh = (const u16*)(ws + WS_Q) + (size_t)h * SEQ * DH;
  const u16* Kh = (const u16*)(ws + WS_K) + (size_t)h * SEQ * DH;
  const u16* Vt = (const u16*)(ws + WS_VT) + (size_t)h * 64 * 512;
#pragma unroll
  for (int i = 0; i < 4; ++i) {
    const int lt = w * 4 + i;
    int gt = 8 * g - 8 + lt; gt = gt < 0 ? 0 : gt;
    __builtin_amdgcn_global_load_lds((GLB_AS const void*)(Kh + (size_t)gt * 512 + lane * 8),
                                     (LDS_AS void*)(lds + lt * 1024), 16, 0, 0);
    __builtin_amdgcn_global_load_lds((GLB_AS const void*)(Vt + (size_t)gt * 512 + lane * 8),
                                     (LDS_AS void*)(lds + 16384 + lt * 1024), 16, 0, 0);
  }
  const int qb0 = 8 * g + w, qb1 = 8 * g + w + 4;
  const bf16x8 q0 = *(const bf16x8*)(Qh + (size_t)(32 * qb0 + c) * DH + 8 * hh);
  const bf16x8 q1 = *(const bf16x8*)(Qh + (size_t)(32 * qb1 + c) * DH + 8 * hh);
  asm volatile("s_waitcnt vmcnt(0)" ::: "memory");
  __syncthreads();
  STAMP(1, 1);
  attn_task(lds, w, qb0, q0, h, ws);
  STAMP(1, 2);
  attn_task(lds, w + 4, qb1, q1, h, ws);
}

__global__ __launch_bounds__(256, 2) void k2_attn_conv(const u16* __restrict__ ssmk,
                                                       const u16* __restrict__ ssmD,
                                                       char* __restrict__ ws) {
  __shared__ __attribute__((aligned(16))) char lds[K2_LDS];
  const int b = blockIdx.x;
  STAMP(1, 0);
  if (b >= 256) {
    const int a = b - 256;
    attn_body(a >> 3, a & 7, lds, ws);
  } else {
    ConvRegs R0, R1;
    conv_load(R0, 2 * b, ssmk, ws);
    conv_load(R1, 2 * b + 1, ssmk, ws);
    conv_compute(R0, 2 * b, lds, ssmD, ws);
    STAMP(1, 2);
    conv_compute(R1, 2 * b + 1, lds, ssmD, ws);
  }
  STAMP(1, 3);
}

__global__ __launch_bounds__(256, 1) void k3_outproj(char* __restrict__ ws, const u16* __restrict__ Aq, u16* __restrict__ out) {
  __shared__ __attribute__((aligned(16))) char lds[131072];
  const int tid = threadIdx.x, lane = tid & 63;
  const int w = __builtin_amdgcn_readfirstlane(tid >> 6);
  const int c = lane & 31, hh = lane >> 5;
  const int b = blockIdx.x;
  STAMP(2, 0);
  const int st = (b & 7) * 4 + (b >> 6), ntl = (b >> 3) & 7;
  {
    const u16* src = (w < 2) ? (const u16*)(ws + WS_WOT) + (size_t)(ntl * 2 + w) * 16384
                             : (const u16*)(ws + WS_O) + (size_t)(st * 2 + w - 2) * 16384;
    src += lane * 8;
#pragma unroll
    for (int i = 0; i < 32; ++i)
      __builtin_amdgcn_global_load_lds((GLB_AS const void*)(src + i * 512), (LDS_AS void*)(lds + w * 32768 + i * 1024), 16, 0, 0);
  }
  asm volatile("" ::: "memory");
  const int ntile = ntl * 2 + (w & 1), stile = st * 2 + (w >> 1);
  const int n0 = ntile * 32, s0 = stile * 32;
  const int s = s0 + c;
  const float g = ((const float*)(ws + WS_G))[s];
  const u16* aT = (const u16*)(ws + WS_AT);
  unsigned av[16];
#pragma unroll
  for (int i = 0; i < 16; ++i) av[i] = aT[(size_t)(n0 + (i & 3) + 8 * (i >> 2) + 4 * hh) * SEQ + s];
  u32x2 qv[4];
#pragma unroll
  for (int gq = 0; gq < 4; ++gq) qv[gq] = *(const u32x2*)(Aq + (size_t)s * DM + n0 + 8 * gq + 4 * hh);
  asm volatile("" ::: "memory");
  f32x16 acc;
#pragma unroll
  for (int i = 0; i < 16; ++i) acc[i] = 0.f;
  const char* ab = lds + (w & 1) * 32768 + lane * 16;
  const char* bb = lds + (2 + (w >> 1)) * 32768 + lane * 16;
#pragma unroll
  for (int ch = 0; ch < 4; ++ch) {
    if (ch == 0) asm volatile("s_waitcnt vmcnt(45)" ::: "memory");
    if (ch == 1) asm volatile("s_waitcnt vmcnt(37)" ::: "memory");
    if (ch == 2) asm volatile("s_waitcnt vmcnt(29)" ::: "memory");
    if (ch == 3) asm volatile("s_waitcnt vmcnt(21)" ::: "memory");
    __builtin_amdgcn_s_barrier();
    asm volatile("" ::: "memory");
    bf16x8 af[8], bf[8];
#pragma unroll
    for (int ks = 0; ks < 8; ++ks) {
      af[ks] = *(const bf16x8*)(ab + (ch * 8 + ks) * 1024);
      bf[ks] = *(const bf16x8*)(bb + (ch * 8 + ks) * 1024);
    }
    __builtin_amdgcn_sched_barrier(0);
#pragma unroll
    for (int ks = 0; ks < 8; ++ks) acc = MFMA(af[ks], bf[ks], acc);
    __builtin_amdgcn_sched_barrier(0);
  }
  STAMP(2, 2);
  const float omg = rbf(1.f - g);
#pragma unroll
  for (int gq = 0; gq < 4; ++gq) {
    const int n = n0 + 8 * gq + 4 * hh;
    float res[4];
#pragma unroll
    for (int i = 0; i < 4; ++i) {
      const float qf = (i & 1) ? bfhi(qv[gq][i >> 1]) : bflo(qv[gq][i >> 1]);
      const float a = rbf(qf * bf2f(av[4 * gq + i]));
      const float bb2 = rbf(acc[4 * gq + i]);
      res[i] = rbf(g * a) + rbf(omg * bb2);
    }
    u32x2 wv;
    wv[0] = pk2(res[0], res[1]); wv[1] = pk2(res[2], res[3]);
    *(u32x2*)(out + (size_t)s * DM + n) = wv;
  }
  STAMP(2, 3);
}

extern "C" void kernel_launch(void* const* d_in, const int* in_sizes, int n_in,
                              void* d_out, int out_size, void* d_ws, size_t ws_size,
                              hipStream_t stream) {
  const u16* Aq = (const u16*)d_in[0];
  const u16* Ak = (const u16*)d_in[1];
  const u16* Av = (const u16*)d_in[2];
  const u16* shiftk = (const u16*)d_in[3];
  const u16* ssmk = (const u16*)d_in[4];
  const u16* ssmD = (const u16*)d_in[5];
  const u16* wqkv = (const u16*)d_in[7];
  const u16* wo = (const u16*)d_in[8];
  const u16* gatew = (const u16*)d_in[9];
  char* ws = (char*)d_ws;
  k1_gemm_prep<<<256, 256, 0, stream>>>(Aq, Ak, Av, shiftk, wqkv, wo, gatew, ws);
  k2_attn_conv<<<512, 256, 0, stream>>>(ssmk, ssmD, ws);
  k3_outproj<<<256, 256, 0, stream>>>(ws, Aq, (u16*)d_out);
}
```

```cpp
#include <hip/hip_runtime.h>
#include <hip/hip_bf16.h>
#include <stdint.h>

#define SEQ 2048
#define DM 512
#define NH 32
#define DH 16

typedef __attribute__((ext_vector_type(8))) short bf16x8;
typedef __attribute__((ext_vector_type(4))) short s16x4;
typedef __attribute__((ext_vector_type(16))) float f32x16;
typedef __attribute__((ext_vector_type(4))) float f32x4;
typedef __attribute__((ext_vector_type(4))) unsigned u32x4;
typedef __attribute__((ext_vector_type(2))) unsigned u32x2;
typedef __attribute__((ext_vector_type(2))) __bf16 bf16x2_t;
typedef unsigned short u16;

#define DI __device__ __forceinline__
#define MFMA(a, b, c) __builtin_amdgcn_mfma_f32_32x32x16_bf16((a), (b), (c), 0, 0, 0)
#define LDS_AS __attribute__((address_space(3)))
#define GLB_AS __attribute__((address_space(1)))

DI float bf2f(unsigned v16) { return __uint_as_float(v16 << 16); }
DI float bflo(unsigned w) { return __uint_as_float(w << 16); }
DI float bfhi(unsigned w) { return __uint_as_float(w & 0xffff0000u); }
DI unsigned pk2(float lo, float hi) {
  bf16x2_t v; v[0] = (__bf16)lo; v[1] = (__bf16)hi;
  return __builtin_bit_cast(unsigned, v);
}
DI float rbf(float x) {
  bf16x2_t v; v[0] = (__bf16)x; v[1] = (__bf16)0.f;
  return __uint_as_float(__builtin_bit_cast(unsigned, v) << 16);
}

#define ST_MOD "sc1"
DI void st16(void* p, u32x4 v) {
  *(u32x4*)p = v;
}
DI void st8(void* p, u32x2 v) {
  *(u32x2*)p = v;
}
#define STAMP(kid, slot)
#define WS_Q   (0u)
#define WS_K   (2u << 20)
#define WS_VT  (4u << 20)
#define WS_KVT (6u << 20)
#define WS_QT  (8u << 20)
#define WS_AT  (10u << 20)
#define WS_O   (12u << 20)
#define WS_WOT (14u << 20)
#define WS_G   (15u << 20)

#define K1_WBYTES (512 * 192)
#define K1_XW 16384

DI const char* uni(const void* p) {
  const uint64_t v = (uint64_t)p;
  const unsigned lo = __builtin_amdgcn_readfirstlane((unsigned)v), hi = __builtin_amdgcn_readfirstlane((unsigned)(v >> 32));
  return (const char*)(((uint64_t)hi << 32) | lo);
}
DI void glds16(unsigned voff, const void* sbase, unsigned ldsaddr) {
  asm volatile("s_mov_b32 m0, %2\n\ts_nop 0\n\tglobal_load_lds_dwordx4 %0, %1"
               :: "v"(voff), "s"(uni(sbase)), "s"(__builtin_amdgcn_readfirstlane(ldsaddr)) : "memory");
}
DI void k1_issue_w(const u16* __restrict__ W, unsigned ldsb, int half, int n0, int w, const unsigned (&wo3)[3], int rot) {
#pragma unroll
  for (int i = 0; i < 4; ++i) {
    const int g = half * 16 + ((4 * i + w + rot) & 15);
    const char* sb = (const char*)W + (size_t)g * (16 * 3072) + n0 * 2;
#pragma unroll
    for (int j = 0; j < 3; ++j) glds16(wo3[j], sb, ldsb + g * 3072 + j * 1024);
  }
}
DI void k1_issue_x(const u16* __restrict__ X, unsigned xb, int half, int m0, unsigned xo) {
#pragma unroll
  for (int i = 0; i < 16; ++i) {
    const char* sb = (const char*)X + (size_t)(m0 + 2 * i) * 1024 + half * 512;
    glds16(xo ^ (unsigned)(((2 * i) & 15) << 4), sb, xb + i * 1024);
  }
}

template <int OFF>
DI void tr_rd(s16x4& d, unsigned base) {
  asm volatile("ds_read_b64_tr_b16 %0, %1 offset:%2" : "=v"(d) : "v"(base), "i"(OFF));
}
template <int KS>
DI void k1_rdb(s16x4 (&t)[6], unsigned base) {
  tr_rd<KS * 3072 + 0>(t[0], base);   tr_rd<KS * 3072 + 768>(t[1], base);
  tr_rd<KS * 3072 + 64>(t[2], base);  tr_rd<KS * 3072 + 64 + 768>(t[3], base);
  tr_rd<KS * 3072 + 128>(t[4], base); tr_rd<KS * 3072 + 128 + 768>(t[5], base);
}
template <int N>
DI void lgkm_wait6(s16x4 (&t)[6]) {
  asm volatile("s_waitcnt lgkmcnt(%6)" : "+v"(t[0]), "+v"(t[1]), "+v"(t[2]), "+v"(t[3]), "+v"(t[4]), "+v"(t[5]) : "i"(N));
}
template <int NV, int KS>
DI void k1_steps(const bf16x8 (&af)[16], unsigned wb, f32x16 (&acc)[3], s16x4 (&t0)[6], s16x4 (&t1)[6], s16x4 (&t2)[6]) {
  if constexpr (KS + 1 < 16) lgkm_wait6<6>(t0); else lgkm_wait6<0>(t0);
#pragma unroll
  for (int j = 0; j < 3; ++j) {
    const bf16x8 bfr = __builtin_shufflevector(t0[2 * j], t0[2 * j + 1], 0, 1, 2, 3, 4, 5, 6, 7);
    if (j >= 3 - NV) acc[j] = MFMA(af[KS], bfr, acc[j]);
    else             acc[j] = MFMA(bfr, af[KS], acc[j]);
    if (j == 0) { if constexpr (KS + 2 < 16) k1_rdb<KS + 2>(t2, wb); }
  }
  __builtin_amdgcn_sched_barrier(0);
  if constexpr (KS + 1 < 16) k1_steps<NV, KS + 1>(af, wb, acc, t1, t2, t0);
}
template <int NV>
DI void k1_half(const bf16x8 (&af)[16], unsigned wb, f32x16 (&acc)[3]) {
  s16x4 t0[6], t1[6], t2[6];
  k1_rdb<0>(t0, wb);
  k1_rdb<1>(t1, wb);
  k1_steps<NV, 0>(af, wb, acc, t0, t1, t2);
}

struct PrepIn { unsigned kk[11], vv[8], wv[4]; u32x4 skw, gw, gx[2]; };
struct PrepOut { u32x4 kv[2]; u32x2 wo; float gv[2]; };
DI void prep_store(const PrepOut& O, char* __restrict__ ws, int b, int pw, int lane);
DI void prep_wait(PrepIn& P);
struct PrepSrc { const u16 *Ak, *Av, *shiftk, *wo, *gatew; };
DI void prep_load(PrepIn& P, const u16* __restrict__ Aq, const u16* __restrict__ Ak, const u16* __restrict__ Av,
                  const u16* __restrict__ shiftk, const u16* __restrict__ wo, const u16* __restrict__ gatew,
                  int b, int pw, int lane);
DI void prep_compute(PrepIn& P, PrepOut& O, int b, int lane);

template <int NV>
DI void gemm_body(const u16* __restrict__ X, char* lds, int mbase, int n0,
                  int w, int lane, u16* __restrict__ Qh, u16* __restrict__ Kh,
                  u16* __restrict__ Vt, char* ws, const PrepSrc& S, int b) {
  const int r = lane & 31, hh = lane >> 5;
  f32x16 acc[3];
#pragma unroll
  for (int j = 0; j < 3; ++j)
#pragma unroll
    for (int i = 0; i < 16; ++i) acc[j][i] = 0.f;

  const int g = (lane >> 4) & 1, q = (lane & 15) >> 2, p = lane & 3;
  const unsigned ldsb = (unsigned)(size_t)lds;
  const unsigned wb = ldsb + (8 * hh + q) * 192 + (16 * g + 4 * p) * 2;
  const unsigned xb = ldsb + K1_WBYTES + w * K1_XW;
  const unsigned xo = (unsigned)(hh * 1024 + ((r ^ hh) << 4));
  const unsigned xa = ldsb + K1_WBYTES + w * K1_XW + r * 512;
  const int m0 = mbase + 32 * w;

  bf16x8 af[16];
  asm volatile("s_waitcnt vmcnt(12)" ::: "memory");
  __builtin_amdgcn_s_barrier();
  asm volatile("" ::: "memory");
  STAMP(0, 1);
#pragma unroll
  for (int ks = 0; ks < 16; ++ks) af[ks] = *(const LDS_AS bf16x8*)(xa + (((2 * ks + hh) ^ (r & 15)) * 16));
  asm volatile("s_waitcnt lgkmcnt(0)" ::: "memory");
  k1_issue_x(X, xb, 1, m0, xo);
  PrepIn P;
  prep_load(P, X, S.Ak, S.Av, S.shiftk, S.wo, S.gatew, b, w, lane);
  asm volatile("" ::: "memory");
  k1_half<NV>(af, wb, acc);
  STAMP(0, 5);
  prep_wait(P);
  PrepOut O;
  prep_compute(P, O, b, lane);
  __builtin_amdgcn_sched_barrier(0);
  STAMP(0, 6);
  asm volatile("s_waitcnt vmcnt(0)" ::: "memory");
  __builtin_amdgcn_s_barrier();
  asm volatile("" ::: "memory");
  STAMP(0, 4);
  prep_store(O, ws, b, w, lane);
#pragma unroll
  for (int ks = 0; ks < 16; ++ks) af[ks] = *(const LDS_AS bf16x8*)(xa + (((2 * ks + hh) ^ (r & 15)) * 16));
  asm volatile("s_waitcnt lgkmcnt(0)" ::: "memory");
  k1_half<NV>(af, wb + 256 * 192, acc);
  STAMP(0, 2);
#pragma unroll
  for (int j = 0; j < 3; ++j) {
    const int nc = n0 + 32 * j;
    if (j >= 3 - NV) {
      const int nv = nc - 1024 + r;
      const int head = nv >> 4, d = nv & 15;
      u16* dst = Vt + (size_t)(head * 64 + (m0 >> 5)) * 512 + d * 16 + 8 * hh;
      u32x4 w0, w1;
      w0[0] = pk2(acc[j][0], acc[j][1]);  w0[1] = pk2(acc[j][2], acc[j][3]);
      w0[2] = pk2(acc[j][4], acc[j][5]);  w0[3] = pk2(acc[j][6], acc[j][7]);
      w1[0] = pk2(acc[j][8], acc[j][9]);  w1[1] = pk2(acc[j][10], acc[j][11]);
      w1[2] = pk2(acc[j][12], acc[j][13]); w1[3] = pk2(acc[j][14], acc[j][15]);
      st16(dst, w0);
      st16(dst + 256, w1);
    } else {
      u16* base = (nc < 512) ? Qh : Kh;
      const int head0 = (nc & 511) >> 4;
#pragma unroll
      for (int e = 0; e < 2; ++e) {
        unsigned a0 = pk2(acc[j][8 * e], acc[j][8 * e + 1]), a1 = pk2(acc[j][8 * e + 2], acc[j][8 * e + 3]);
        unsigned b0 = pk2(acc[j][8 * e + 4], acc[j][8 * e + 5]), b1 = pk2(acc[j][8 * e + 6], acc[j][8 * e + 7]);
        auto r0 = __builtin_amdgcn_permlane32_swap(a0, b0, false, false);
        auto r1 = __builtin_amdgcn_permlane32_swap(a1, b1, false, false);
        u32x4 wv = {r0[0], r1[0], r0[1], r1[1]};
        u16* dst = base + ((size_t)(head0 + e) * SEQ + m0 + r) * DH + 8 * hh;
        st16(dst, wv);
      }
    }
  }
}

#define GLD1(dst, voff, sb, imm) asm volatile("global_load_dword %0, %1, %2 offset:" #imm : "=v"(dst) : "v"(voff), "s"(uni(sb)))
#define GLD4(dst, voff, sb, imm) asm volatile("global_load_dwordx4 %0, %1, %2 offset:" #imm : "=v"(dst) : "v"(voff), "s"(uni(sb)))
#define GLDH(dst, voff, sb, imm) asm volatile("global_load_ushort %0, %1, %2 offset:" #imm : "=v"(dst) : "v"(voff), "s"(uni(sb)))
DI void prep_load(PrepIn& P, const u16* __restrict__ Aq, const u16* __restrict__ Ak, const u16* __restrict__ Av,
                  const u16* __restrict__ shiftk, const u16* __restrict__ wo, const u16* __restrict__ gatew,
                  int b, int pw, int lane) {
  const int st = b >> 3, dt = b & 7;
  const int dg = lane & 7, sg = lane >> 3;
  const int d0 = dt * 64 + dg * 8 + 2 * pw;
  const int s0 = st * 64 + sg * 8;
  const unsigned va = (unsigned)((s0 == 0 ? 0 : s0 - 3) * 1024 + d0 * 2);
  const unsigned vb = (unsigned)(s0 * 1024 + d0 * 2);
  const char* k2 = (const char*)Ak + 4096;
  const char* v2 = (const char*)Av + 4096;
  GLD1(P.kk[0], va, Ak, 0); GLD1(P.kk[1], va, Ak, 1024); GLD1(P.kk[2], va, Ak, 2048);
  GLD1(P.kk[3], vb, Ak, 0); GLD1(P.kk[4], vb, Ak, 1024); GLD1(P.kk[5], vb, Ak, 2048); GLD1(P.kk[6], vb, Ak, 3072);
  GLD1(P.kk[7], vb, k2, 0); GLD1(P.kk[8], vb, k2, 1024); GLD1(P.kk[9], vb, k2, 2048); GLD1(P.kk[10], vb, k2, 3072);
  GLD1(P.vv[0], vb, Av, 0); GLD1(P.vv[1], vb, Av, 1024); GLD1(P.vv[2], vb, Av, 2048); GLD1(P.vv[3], vb, Av, 3072);
  GLD1(P.vv[4], vb, v2, 0); GLD1(P.vv[5], vb, v2, 1024); GLD1(P.vv[6], vb, v2, 2048); GLD1(P.vv[7], vb, v2, 3072);
  const unsigned vs = (unsigned)(d0 * 8), vl = (unsigned)(lane * 16), vh = (unsigned)(lane * 2);
  GLD4(P.skw, vs, shiftk, 0);
  GLD4(P.gw, vl, gatew, 0);
  const char* gq = (const char*)Aq + (size_t)(b * 8 + pw * 2) * 1024;
  GLD4(P.gx[0], vl, gq, 0); GLD4(P.gx[1], vl, gq, 1024);
  const char* wr = (const char*)wo + (size_t)((b >> 3) * 16 + pw * 4) * 1024 + (b & 7) * 128;
  GLDH(P.wv[0], vh, wr, 0); GLDH(P.wv[1], vh, wr, 1024); GLDH(P.wv[2], vh, wr, 2048); GLDH(P.wv[3], vh, wr, 3072);
}
DI void prep_wait(PrepIn& P) {
  __builtin_amdgcn_sched_barrier(0);
  asm volatile("s_waitcnt vmcnt(0)" ::: "memory");
  __builtin_amdgcn_sched_barrier(0);
  asm volatile(""
               : "+v"(P.kk[0]), "+v"(P.kk[1]), "+v"(P.kk[2]), "+v"(P.kk[3]), "+v"(P.kk[4]), "+v"(P.kk[5]),
                 "+v"(P.kk[6]), "+v"(P.kk[7]), "+v"(P.kk[8]), "+v"(P.kk[9]), "+v"(P.kk[10]),
                 "+v"(P.vv[0]), "+v"(P.vv[1]), "+v"(P.vv[2]), "+v"(P.vv[3]), "+v"(P.vv[4]), "+v"(P.vv[5]),
                 "+v"(P.vv[6]), "+v"(P.vv[7]));
  asm volatile("" : "+v"(P.skw), "+v"(P.gw), "+v"(P.gx[0]), "+v"(P.gx[1]),
                    "+v"(P.wv[0]), "+v"(P.wv[1]), "+v"(P.wv[2]), "+v"(P.wv[3]));
}

DI void prep_compute(PrepIn& P, PrepOut& O, int b, int lane) {
  const int s0 = (b >> 3) * 64 + (lane >> 3) * 8;
  if (s0 == 0) { P.kk[0] = 0; P.kk[1] = 0; P.kk[2] = 0; }
#pragma unroll
  for (int c = 0; c < 2; ++c) {
    const unsigned s01 = P.skw[c * 2], s23 = P.skw[c * 2 + 1];
    const float sk0 = bflo(s01), sk1 = bfhi(s01), sk2 = bflo(s23), sk3 = bfhi(s23);
    float kf[11];
#pragma unroll
    for (int i = 0; i < 11; ++i) kf[i] = c ? bfhi(P.kk[i]) : bflo(P.kk[i]);
    float kvv[8];
#pragma unroll
    for (int i = 0; i < 8; ++i) {
      float acc = rbf(kf[i] * sk3);
      acc = rbf(acc + rbf(kf[i + 1] * sk2));
      acc = rbf(acc + rbf(kf[i + 2] * sk1));
      acc = rbf(acc + rbf(kf[i + 3] * sk0));
      const float vf = c ? bfhi(P.vv[i]) : bflo(P.vv[i]);
      kvv[i] = acc * vf;
    }
    O.kv[c][0] = pk2(kvv[0], kvv[1]); O.kv[c][1] = pk2(kvv[2], kvv[3]);
    O.kv[c][2] = pk2(kvv[4], kvv[5]); O.kv[c][3] = pk2(kvv[6], kvv[7]);
  }
  O.wo[0] = P.wv[0] | (P.wv[1] << 16); O.wo[1] = P.wv[2] | (P.wv[3] << 16);
#pragma unroll
  for (int i = 0; i < 2; ++i) {
    float sum = 0.f;
#pragma unroll
    for (int e = 0; e < 4; ++e) {
      sum += rbf(bflo(P.gx[i][e]) * bflo(P.gw[e]));
      sum += rbf(bfhi(P.gx[i][e]) * bfhi(P.gw[e]));
    }
#pragma unroll
    for (int o = 32; o > 0; o >>= 1) sum += __shfl_xor(sum, o);
    const float z = rbf(sum);
    O.gv[i] = rbf(1.f / (1.f + __expf(-z)));
  }
}

DI void prep_store(const PrepOut& O, char* __restrict__ ws, int b, int pw, int lane) {
  const int st = b >> 3, dt = b & 7;
  const int dg = lane & 7, sg = lane >> 3;
  const int d0 = dt * 64 + dg * 8 + 2 * pw;
  const int s0 = st * 64 + sg * 8;
  u16* kvT = (u16*)(ws + WS_KVT);
  st16(kvT + (size_t)d0 * SEQ + s0, O.kv[0]);
  st16(kvT + (size_t)(d0 + 1) * SEQ + s0, O.kv[1]);
  const int wk0 = (b >> 3) * 16 + pw * 4, wn = (b & 7) * 64 + lane;
  u16* woT = (u16*)(ws + WS_WOT);
  const size_t off = ((size_t)(((wn >> 5) * 32 + (wk0 >> 4)) * 2 + ((wk0 >> 3) & 1)) * 32 + (wn & 31)) * 8 + (wk0 & 7);
  st8(woT + off, O.wo);
  float* gout = (float*)(ws + WS_G);
  if (lane < 2) gout[b * 8 + pw * 2 + lane] = lane ? O.gv[1] : O.gv[0];
}

__global__ __launch_bounds__(256, 1) void k1_gemm_prep(
    const u16* __restrict__ Aq, const u16* __restrict__ Ak, const u16* __restrict__ Av,
    const u16* __restrict__ shiftk, const u16* __restrict__ wqkv, const u16* __restrict__ wo,
    const u16* __restrict__ gatew, char* __restrict__ ws) {
  __shared__ __attribute__((aligned(16))) char lds[K1_WBYTES + 4 * K1_XW];
  const int tid = threadIdx.x, lane = tid & 63;
  const int w = __builtin_amdgcn_readfirstlane(tid >> 6);
  const int b = blockIdx.x;
  STAMP(0, 0);
  const int xcd = b & 7, idx = b >> 3;
  const int mt = (xcd >> 1) * 4 + (idx >> 3);
  const int nt = (xcd & 1) * 8 + (idx & 7);
  const int n0 = nt * 96, mbase = mt * 128;
  const int rot = (idx >> 3) * 4;
  const unsigned ldsb = (unsigned)(size_t)lds;
  unsigned wo3[3];
#pragma unroll
  for (int j = 0; j < 3; ++j) {
    const int ci = j * 64 + lane, row = ci / 12, c16 = ci - row * 12;
    wo3[j] = (unsigned)(row * 3072 + c16 * 16);
  }
  const unsigned xo = (unsigned)((lane >> 5) * 1024 + (((lane & 31) ^ (lane >> 5)) << 4));

  const PrepSrc P = {Ak, Av, shiftk, wo, gatew};
  k1_issue_w(wqkv, ldsb, 0, n0, w, wo3, rot);
  k1_issue_x(Aq, ldsb + K1_WBYTES + w * K1_XW, 0, mbase + 32 * w, xo);
  k1_issue_w(wqkv, ldsb, 1, n0, w, wo3, rot);

  u16* Qh = (u16*)(ws + WS_Q);
  u16* Kh = (u16*)(ws + WS_K);
  u16* Vt = (u16*)(ws + WS_VT);
  if (nt <= 9) gemm_body<0>(Aq, lds, mbase, n0, w, lane, Qh, Kh, Vt, ws, P, b);
  else if (nt == 10) gemm_body<1>(Aq, lds, mbase, n0, w, lane, Qh, Kh, Vt, ws, P, b);
  else gemm_body<3>(Aq, lds, mbase, n0, w, lane, Qh, Kh, Vt, ws, P, b);
  STAMP(0, 3);
}

#define CSTR 4608
#define XR_OFF (8 * CSTR)
#define XR_STR 80
#define K2_LDS (XR_OFF + 95 * XR_STR)

struct ConvRegs { u32x4 h0, h1, hx0, hx1, xv; };

DI void conv_load(ConvRegs& R, int d, const u16* __restrict__ ssmk, char* __restrict__ ws) {
  const int tid = threadIdx.x, lane = tid & 63, w = tid >> 6;
  const int r = lane & 31, hh = lane >> 5;
  const u16* hrow = ssmk + (size_t)d * SEQ;
  const u32x4 z4 = {0u, 0u, 0u, 0u};
  R.h0 = *(const u32x4*)(hrow + 8 * (tid >= 4 ? tid - 4 : 0));
  R.h1 = *(const u32x4*)(hrow + 8 * (tid >= 3 ? tid - 3 : 0));
  if (tid < 4) R.h0 = z4;
  if (tid < 3) R.h1 = z4;
  R.hx0 = z4; R.hx1 = z4;
  if (tid < 4) {
    R.hx0 = *(const u32x4*)(hrow + 8 * (252 + tid));
    if (tid < 3) R.hx1 = *(const u32x4*)(hrow + 8 * (253 + tid));
  }
  R.xv = *(const u32x4*)((const u16*)(ws + WS_KVT) + (size_t)d * SEQ + 8 * tid);
}

DI void conv_write_copies(char* lds, int t, u32x4 h0, u32x4 h1) {
  unsigned wv[8] = {h0[0], h0[1], h0[2], h0[3], h1[0], h1[1], h1[2], h1[3]};
  unsigned od[7];
#pragma unroll
  for (int i = 0; i < 7; ++i) od[i] = __builtin_amdgcn_alignbyte(wv[i + 1], wv[i], 2);
#pragma unroll
  for (int c = 0; c < 8; ++c) {
    const int e = c >> 1;
    u32x4 v;
    if (c & 1) { v[0] = od[e]; v[1] = od[e + 1]; v[2] = od[e + 2]; v[3] = od[e + 3]; }
    else       { v[0] = wv[e]; v[1] = wv[e + 1]; v[2] = wv[e + 2]; v[3] = wv[e + 3]; }
    const unsigned off = (0x5D9159D0u >> (4 * c)) & 15u;
    *(u32x4*)(lds + c * CSTR + off * 16 + t * 16) = v;
  }
}

DI void conv_compute(const ConvRegs& R, int d, char* lds, const u16* __restrict__ ssmD,
                     char* __restrict__ ws) {
  const int tid = threadIdx.x, lane = tid & 63, w = tid >> 6;
  const int r = lane & 31, hh = lane >> 5;
  {
    const u32x4 z4 = {0u, 0u, 0u, 0u};
    conv_write_copies(lds, tid, R.h0, R.h1);
    if (tid < 4) conv_write_copies(lds, 256 + tid, R.hx0, R.hx1);
    u32x4 rv;
#pragma unroll
    for (int i = 0; i < 4; ++i) rv[i] = __builtin_amdgcn_alignbit(R.xv[3 - i], R.xv[3 - i], 16);
    *(u32x4*)(lds + XR_OFF + ((tid >> 2) + 31) * XR_STR + (3 - (tid & 3)) * 16) = rv;
    if (tid < 124) *(u32x4*)(lds + XR_OFF + (tid >> 2) * XR_STR + (tid & 3) * 16) = z4;
  }
  __syncthreads();
  if (!(d & 1)) STAMP(1, 1); else STAMP(1, 4);

  f32x16 acc0, acc1a, acc1b;
#pragma unroll
  for (int i = 0; i < 16; ++i) { acc0[i] = 0.f; acc1a[i] = 0.f; acc1b[i] = 0.f; }
  {
    const int c = (1 + r) & 7, u = (1 + r) >> 3;
    const unsigned off = (0x5D9159D0u >> (4 * c)) & 15u;
    const char* abase = lds + c * CSTR + off * 16 + (16 + 8 * hh + 8 * u) * 2 + w * 8 * 64;
    const char* bbase = lds + XR_OFF + (r + 31 - w * 8) * XR_STR + 16 * hh;
#pragma unroll
    for (int gi = 0; gi < 8; ++gi) {
#pragma unroll
      for (int ks = 0; ks < 2; ++ks) {
        const bf16x8 A0 = *(const bf16x8*)(abase + gi * 64 - ks * 32);
        const bf16x8 A1 = *(const bf16x8*)(abase + gi * 64 - ks * 32 + 32 * 64);
        const bf16x8 B0 = *(const bf16x8*)(bbase - gi * XR_STR + (1 - ks) * 32);
        const bf16x8 B1 = *(const bf16x8*)(bbase + (32 - gi) * XR_STR + (1 - ks) * 32);
        acc0 = MFMA(A0, B0, acc0);
        acc1a = MFMA(A1, B0, acc1a);
        acc1b = MFMA(A0, B1, acc1b);
      }
    }
  }
  __syncthreads();
  if (!(d & 1)) STAMP(1, 5); else STAMP(1, 6);
  {
    float* red = (float*)lds;
#pragma unroll
    for (int dst = 0; dst < 4; ++dst)
#pragma unroll
      for (int hf = 0; hf < 2; ++hf) {
        f32x4 v;
#pragma unroll
        for (int i = 0; i < 4; ++i) {
          const int rg = 8 * (dst & 1) + 4 * hf + i;
          v[i] = (dst >> 1) ? (acc1a[rg] + acc1b[rg]) : acc0[rg];
        }
        *(f32x4*)(red + ((((w * 4 + dst) * 2 + hf) * 64) + lane) * 4) = v;
      }
  }
  __syncthreads();
  {
    const float* red = (const float*)lds;
    const float Dv = bf2f(ssmD[d]);
    u16* aT = (u16*)(ws + WS_AT) + (size_t)d * SEQ;
    const int ablk = 32 * (w >> 1) + r;
#pragma unroll
    for (int hf = 0; hf < 2; ++hf) {
      f32x4 y = {0.f, 0.f, 0.f, 0.f};
#pragma unroll
      for (int src = 0; src < 4; ++src) {
        const f32x4 v = *(const f32x4*)(red + ((((src * 4 + w) * 2 + hf) * 64) + lane) * 4);
        y += v;
      }
      const int b0 = 16 * (w & 1) + 8 * hf + 4 * hh;
      const int s = 32 * ablk + b0;
      const u32x2 xv = *(const u32x2*)(lds + XR_OFF + (ablk + 31) * XR_STR + (28 - b0) * 2);
      float kv[4] = {bfhi(xv[1]), bflo(xv[1]), bfhi(xv[0]), bflo(xv[0])};
      float a[4];
#pragma unroll
      for (int i = 0; i < 4; ++i) {
        a[i] = rbf(y[i]) + rbf(kv[i] * Dv);
      }
      u32x2 o;
      o[0] = pk2(a[0], a[1]); o[1] = pk2(a[2], a[3]);
      st8(aT + s, o);
    }
  }
  __syncthreads();
}

DI void attn_task(const char* lds, int lt0, int qb, bf16x8 qf, int h, char* __restrict__ ws) {
  const int lane = threadIdx.x & 63;
  const int c = lane & 31, hh = lane >> 5;
  f32x16 zero;
#pragma unroll
  for (int i = 0; i < 16; ++i) zero[i] = 0.f;
  f32x16 sc[9];
  const char* kb = lds + lt0 * 1024 + c * 32 + hh * 16;
#pragma unroll
  for (int t = 0; t < 9; ++t) {
    const bf16x8 kf = *(const bf16x8*)(kb + t * 1024);
    sc[t] = MFMA(kf, qf, zero);
  }
  const float NEG = -1e30f;
#pragma unroll
  for (int i = 0; i < 16; ++i) {
    const int row = (i & 3) + 8 * (i >> 2) + 4 * hh;
    sc[0][i] = (row > c) ? sc[0][i] : NEG;
    sc[8][i] = (row <= c) ? sc[8][i] : NEG;
  }
  if (qb < 8) {
#pragma unroll
    for (int t = 0; t < 8; ++t)
      if (qb - 8 + t < 0) {
#pragma unroll
        for (int i = 0; i < 16; ++i) sc[t][i] = NEG;
      }
  }
  float m = NEG;
#pragma unroll
  for (int t = 0; t < 9; ++t)
#pragma unroll
    for (int i = 0; i < 16; ++i) m = fmaxf(m, sc[t][i]);
  m = fmaxf(m, __shfl_xor(m, 32));
  const float cs = 0.25f * 1.4426950408889634f;
  const float mc = m * cs;
  f32x16 oacc[2];
  oacc[0] = zero; oacc[1] = zero;
  const unsigned onesw = (c == 16 || c == 20) ? 0x3F803F80u : 0u;
  const char* vb = lds + 16384 + lt0 * 1024 + (c & 15) * 32 + hh * 16;
#pragma unroll
  for (int t = 0; t < 9; ++t) {
#pragma unroll
    for (int s = 0; s < 2; ++s) {
      u32x4 vw = *(const u32x4*)(vb + t * 1024 + s * 512);
      if (c >= 16) { vw[0] = onesw; vw[1] = onesw; vw[2] = onesw; vw[3] = onesw; }
      u32x4 pw;
#pragma unroll
      for (int e = 0; e < 4; ++e) {
        const float p0 = __builtin_amdgcn_exp2f(fmaf(sc[t][8 * s + 2 * e], cs, -mc));
        const float p1 = __builtin_amdgcn_exp2f(fmaf(sc[t][8 * s + 2 * e + 1], cs, -mc));
        pw[e] = pk2(p0, p1);
      }
      oacc[s] = MFMA(__builtin_bit_cast(bf16x8, vw), __builtin_bit_cast(bf16x8, pw), oacc[s]);
    }
  }
  const float inv = 1.f / (oacc[0][8] + oacc[1][8]);
  unsigned a0 = pk2((oacc[0][0] + oacc[1][0]) * inv, (oacc[0][1] + oacc[1][1]) * inv);
  unsigned a1 = pk2((oacc[0][2] + oacc[1][2]) * inv, (oacc[0][3] + oacc[1][3]) * inv);
  unsigned b0 = pk2((oacc[0][4] + oacc[1][4]) * inv, (oacc[0][5] + oacc[1][5]) * inv);
  unsigned b1 = pk2((oacc[0][6] + oacc[1][6]) * inv, (oacc[0][7] + oacc[1][7]) * inv);
  auto r0 = __builtin_amdgcn_permlane32_swap(a0, b0, false, false);
  auto r1 = __builtin_amdgcn_permlane32_swap(a1, b1, false, false);
  u32x4 wv = {r0[0], r1[0], r0[1], r1[1]};
  u16* o = (u16*)(ws + WS_O) + ((size_t)((qb * 32 + h) * 2 + hh) * 32 + c) * 8;
  st16(o, wv);
}

DI void attn_body(int h, int g, char* lds, char* __restrict__ ws) {
  const int tid = threadIdx.x, lane = tid & 63;
  const int w = __builtin_amdgcn_readfirstlane(tid >> 6);
  const int c = lane & 31, hh = lane >> 5;
  const u16* Qh = (const u16*)(ws + WS_Q) + (size_t)h * SEQ * DH;
  const u16* Kh = (const u16*)(ws + WS_K) + (size_t)h * SEQ * DH;
  const u16* Vt = (const u16*)(ws + WS_VT) + (size_t)h * 64 * 512;
#pragma unroll
  for (int i = 0; i < 4; ++i) {
    const int lt = w * 4 + i;
    int gt = 8 * g - 8 + lt; gt = gt < 0 ? 0 : gt;
    __builtin_amdgcn_global_load_lds((GLB_AS const void*)(Kh + (size_t)gt * 512 + lane * 8),
                                     (LDS_AS void*)(lds + lt * 1024), 16, 0, 0);
    __builtin_amdgcn_global_load_lds((GLB_AS const void*)(Vt + (size_t)gt * 512 + lane * 8),
                                     (LDS_AS void*)(lds + 16384 + lt * 1024), 16, 0, 0);
  }
  const int qb0 = 8 * g + w, qb1 = 8 * g + w + 4;
  const bf16x8 q0 = *(const bf16x8*)(Qh + (size_t)(32 * qb0 + c) * DH + 8 * hh);
  const bf16x8 q1 = *(const bf16x8*)(Qh + (size_t)(32 * qb1 + c) * DH + 8 * hh);
  asm volatile("s_waitcnt vmcnt(0)" ::: "memory");
  __syncthreads();
  STAMP(1, 1);
  attn_task(lds, w, qb0, q0, h, ws);
  STAMP(1, 2);
  attn_task(lds, w + 4, qb1, q1, h, ws);
}

__global__ __launch_bounds__(256, 2) void k2_attn_conv(const u16* __restrict__ ssmk,
                                                       const u16* __restrict__ ssmD,
                                                       char* __restrict__ ws) {
  __shared__ __attribute__((aligned(16))) char lds[K2_LDS];
  const int b = blockIdx.x;
  STAMP(1, 0);
  if (b >= 256) {
    const int a = b - 256;
    attn_body(a >> 3, a & 7, lds, ws);
  } else {
    ConvRegs R0, R1;
    conv_load(R0, 2 * b, ssmk, ws);
    conv_load(R1, 2 * b + 1, ssmk, ws);
    conv_compute(R0, 2 * b, lds, ssmD, ws);
    STAMP(1, 2);
    conv_compute(R1, 2 * b + 1, lds, ssmD, ws);
  }
  STAMP(1, 3);
}

__global__ __launch_bounds__(256, 1) void k3_outproj(char* __restrict__ ws, const u16* __restrict__ Aq, u16* __restrict__ out) {
  __shared__ __attribute__((aligned(16))) char lds[131072];
  const int tid = threadIdx.x, lane = tid & 63;
  const int w = __builtin_amdgcn_readfirstlane(tid >> 6);
  const int c = lane & 31, hh = lane >> 5;
  const int b = blockIdx.x;
  STAMP(2, 0);
  const int st = (b & 7) * 4 + (b >> 6), ntl = (b >> 3) & 7;
  {
    const u16* src = (w < 2) ? (const u16*)(ws + WS_WOT) + (size_t)(ntl * 2 + w) * 16384
                             : (const u16*)(ws + WS_O) + (size_t)(st * 2 + w - 2) * 16384;
    src += lane * 8;
#pragma unroll
    for (int i = 0; i < 32; ++i)
      __builtin_amdgcn_global_load_lds((GLB_AS const void*)(src + i * 512), (LDS_AS void*)(lds + w * 32768 + i * 1024), 16, 0, 0);
  }
  asm volatile("" ::: "memory");
  const int ntile = ntl * 2 + (w & 1), stile = st * 2 + (w >> 1);
  const int n0 = ntile * 32, s0 = stile * 32;
  const int s = s0 + c;
  const float g = ((const float*)(ws + WS_G))[s];
  const u16* aT = (const u16*)(ws + WS_AT);
  unsigned av[16];
#pragma unroll
  for (int i = 0; i < 16; ++i) av[i] = aT[(size_t)(n0 + (i & 3) + 8 * (i >> 2) + 4 * hh) * SEQ + s];
  u32x2 qv[4];
#pragma unroll
  for (int gq = 0; gq < 4; ++gq) qv[gq] = *(const u32x2*)(Aq + (size_t)s * DM + n0 + 8 * gq + 4 * hh);
  asm volatile("" ::: "memory");
  f32x16 acc;
#pragma unroll
  for (int i = 0; i < 16; ++i) acc[i] = 0.f;
  const char* ab = lds + (w & 1) * 32768 + lane * 16;
  const char* bb = lds + (2 + (w >> 1)) * 32768 + lane * 16;
#pragma unroll
  for (int ch = 0; ch < 4; ++ch) {
    if (ch == 0) asm volatile("s_waitcnt vmcnt(45)" ::: "memory");
    if (ch == 1) asm volatile("s_waitcnt vmcnt(37)" ::: "memory");
    if (ch == 2) asm volatile("s_waitcnt vmcnt(29)" ::: "memory");
    if (ch == 3) asm volatile("s_waitcnt vmcnt(21)" ::: "memory");
    __builtin_amdgcn_s_barrier();
    asm volatile("" ::: "memory");
    bf16x8 af[8], bf[8];
#pragma unroll
    for (int ks = 0; ks < 8; ++ks) {
      af[ks] = *(const bf16x8*)(ab + (ch * 8 + ks) * 1024);
      bf[ks] = *(const bf16x8*)(bb + (ch * 8 + ks) * 1024);
    }
    __builtin_amdgcn_sched_barrier(0);
#pragma unroll
    for (int ks = 0; ks < 8; ++ks) acc = MFMA(af[ks], bf[ks], acc);
    __builtin_amdgcn_sched_barrier(0);
  }
  STAMP(2, 2);
  const float omg = rbf(1.f - g);
#pragma unroll
  for (int gq = 0; gq < 4; ++gq) {
    const int n = n0 + 8 * gq + 4 * hh;
    float res[4];
#pragma unroll
    for (int i = 0; i < 4; ++i) {
      const float qf = (i & 1) ? bfhi(qv[gq][i >> 1]) : bflo(qv[gq][i >> 1]);
      const float a = rbf(qf * bf2f(av[4 * gq + i]));
      const float bb2 = rbf(acc[4 * gq + i]);
      res[i] = rbf(g * a) + rbf(omg * bb2);
    }
    u32x2 wv;
    wv[0] = pk2(res[0], res[1]); wv[1] = pk2(res[2], res[3]);
    st8(out + (size_t)s * DM + n, wv);
  }
  STAMP(2, 3);
}

extern "C" void kernel_launch(void* const* d_in, const int* in_sizes, int n_in,
                              void* d_out, int out_size, void* d_ws, size_t ws_size,
                              hipStream_t stream) {
  const u16* Aq = (const u16*)d_in[0];
  const u16* Ak = (const u16*)d_in[1];
  const u16* Av = (const u16*)d_in[2];
  const u16* shiftk = (const u16*)d_in[3];
  const u16* ssmk = (const u16*)d_in[4];
  const u16* ssmD = (const u16*)d_in[5];
  const u16* wqkv = (const u16*)d_in[7];
  const u16* wo = (const u16*)d_in[8];
  const u16* gatew = (const u16*)d_in[9];
  char* ws = (char*)d_ws;
  k1_gemm_prep<<<256, 256, 0, stream>>>(Aq, Ak, Av, shiftk, wqkv, wo, gatew, ws);
  k2_attn_conv<<<512, 256, 0, stream>>>(ssmk, ssmD, ws);
  k3_outproj<<<256, 256, 0, stream>>>(ws, Aq, (u16*)d_out);
}
```

```cpp
#include <hip/hip_runtime.h>
#include <hip/hip_bf16.h>
#include <stdint.h>

#define SEQ 2048
#define DM 512
#define NH 32
#define DH 16

typedef __attribute__((ext_vector_type(8))) short bf16x8;
typedef __attribute__((ext_vector_type(4))) short s16x4;
typedef __attribute__((ext_vector_type(16))) float f32x16;
typedef __attribute__((ext_vector_type(4))) float f32x4;
typedef __attribute__((ext_vector_type(4))) unsigned u32x4;
typedef __attribute__((ext_vector_type(2))) unsigned u32x2;
typedef __attribute__((ext_vector_type(2))) __bf16 bf16x2_t;
typedef unsigned short u16;

#define DI __device__ __forceinline__
#define MFMA(a, b, c) __builtin_amdgcn_mfma_f32_32x32x16_bf16((a), (b), (c), 0, 0, 0)
#define LDS_AS __attribute__((address_space(3)))
#define GLB_AS __attribute__((address_space(1)))

DI float bf2f(unsigned v16) { return __uint_as_float(v16 << 16); }
DI float bflo(unsigned w) { return __uint_as_float(w << 16); }
DI float bfhi(unsigned w) { return __uint_as_float(w & 0xffff0000u); }
DI unsigned pk2(float lo, float hi) {
  bf16x2_t v; v[0] = (__bf16)lo; v[1] = (__bf16)hi;
  return __builtin_bit_cast(unsigned, v);
}
DI float rbf(float x) {
  bf16x2_t v; v[0] = (__bf16)x; v[1] = (__bf16)0.f;
  return __uint_as_float(__builtin_bit_cast(unsigned, v) << 16);
}

#define ST_MOD "sc1"
DI void st16(void* p, u32x4 v) {
  asm volatile("global_store_dwordx4 %0, %1, off " ST_MOD "\n\ts_nop 1" :: "v"(p), "v"(v) : "memory");
}
DI void st8(void* p, u32x2 v) {
  asm volatile("global_store_dwordx2 %0, %1, off\n\ts_nop 1" :: "v"(p), "v"(v) : "memory");
}
#define STAMP(kid, slot)
#define WS_Q   (0u)
#define WS_K   (2u << 20)
#define WS_VT  (4u << 20)
#define WS_KVT (6u << 20)
#define WS_QT  (8u << 20)
#define WS_AT  (10u << 20)
#define WS_O   (12u << 20)
#define WS_WOT (14u << 20)
#define WS_G   (15u << 20)

#define K1_WBYTES (512 * 192)
#define K1_XW 16384

DI const char* uni(const void* p) {
  const uint64_t v = (uint64_t)p;
  const unsigned lo = __builtin_amdgcn_readfirstlane((unsigned)v), hi = __builtin_amdgcn_readfirstlane((unsigned)(v >> 32));
  return (const char*)(((uint64_t)hi << 32) | lo);
}
DI void glds16(unsigned voff, const void* sbase, unsigned ldsaddr) {
  asm volatile("s_mov_b32 m0, %2\n\ts_nop 0\n\tglobal_load_lds_dwordx4 %0, %1"
               :: "v"(voff), "s"(uni(sbase)), "s"(__builtin_amdgcn_readfirstlane(ldsaddr)) : "memory");
}
DI void k1_issue_w(const u16* __restrict__ W, unsigned ldsb, int half, int n0, int w, const unsigned (&wo3)[3], int rot) {
#pragma unroll
  for (int i = 0; i < 4; ++i) {
    const int g = half * 16 + ((4 * i + w + rot) & 15);
    const char* sb = (const char*)W + (size_t)g * (16 * 3072) + n0 * 2;
#pragma unroll
    for (int j = 0; j < 3; ++j) glds16(wo3[j], sb, ldsb + g * 3072 + j * 1024);
  }
}
DI void k1_issue_x(const u16* __restrict__ X, unsigned xb, int half, int m0, unsigned xo) {
#pragma unroll
  for (int i = 0; i < 16; ++i) {
    const char* sb = (const char*)X + (size_t)(m0 + 2 * i) * 1024 + half * 512;
    glds16(xo ^ (unsigned)(((2 * i) & 15) << 4), sb, xb + i * 1024);
  }
}

template <int OFF>
DI void tr_rd(s16x4& d, unsigned base) {
  asm volatile("ds_read_b64_tr_b16 %0, %1 offset:%2" : "=v"(d) : "v"(base), "i"(OFF));
}
template <int KS>
DI void k1_rdb(s16x4 (&t)[6], unsigned base) {
  tr_rd<KS * 3072 + 0>(t[0], base);   tr_rd<KS * 3072 + 768>(t[1], base);
  tr_rd<KS * 3072 + 64>(t[2], base);  tr_rd<KS * 3072 + 64 + 768>(t[3], base);
  tr_rd<KS * 3072 + 128>(t[4], base); tr_rd<KS * 3072 + 128 + 768>(t[5], base);
}
template <int N>
DI void lgkm_wait6(s16x4 (&t)[6]) {
  asm volatile("s_waitcnt lgkmcnt(%6)" : "+v"(t[0]), "+v"(t[1]), "+v"(t[2]), "+v"(t[3]), "+v"(t[4]), "+v"(t[5]) : "i"(N));
}
template <int NV, int KS>
DI void k1_steps(const bf16x8 (&af)[16], unsigned wb, f32x16 (&acc)[3], s16x4 (&t0)[6], s16x4 (&t1)[6], s16x4 (&t2)[6]) {
  if constexpr (KS + 1 < 16) lgkm_wait6<6>(t0); else lgkm_wait6<0>(t0);
#pragma unroll
  for (int j = 0; j < 3; ++j) {
    const bf16x8 bfr = __builtin_shufflevector(t0[2 * j], t0[2 * j + 1], 0, 1, 2, 3, 4, 5, 6, 7);
    if (j >= 3 - NV) acc[j] = MFMA(af[KS], bfr, acc[j]);
    else             acc[j] = MFMA(bfr, af[KS], acc[j]);
    if (j == 0) { if constexpr (KS + 2 < 16) k1_rdb<KS + 2>(t2, wb); }
  }
  __builtin_amdgcn_sched_barrier(0);
  if constexpr (KS + 1 < 16) k1_steps<NV, KS + 1>(af, wb, acc, t1, t2, t0);
}
template <int NV>
DI void k1_half(const bf16x8 (&af)[16], unsigned wb, f32x16 (&acc)[3]) {
  s16x4 t0[6], t1[6], t2[6];
  k1_rdb<0>(t0, wb);
  k1_rdb<1>(t1, wb);
  k1_steps<NV, 0>(af, wb, acc, t0, t1, t2);
}

struct PrepIn { unsigned kk[11], vv[8], wv[4]; u32x4 skw, gw, gx[2]; };
struct PrepOut { u32x4 kv[2]; u32x2 wo; float gv[2]; };
DI void prep_store(const PrepOut& O, char* __restrict__ ws, int b, int pw, int lane);
DI void prep_wait(PrepIn& P);
struct PrepSrc { const u16 *Ak, *Av, *shiftk, *wo, *gatew; };
DI void prep_load(PrepIn& P, const u16* __restrict__ Aq, const u16* __restrict__ Ak, const u16* __restrict__ Av,
                  const u16* __restrict__ shiftk, const u16* __restrict__ wo, const u16* __restrict__ gatew,
                  int b, int pw, int lane);
DI void prep_compute(PrepIn& P, PrepOut& O, int b, int lane);

template <int NV>
DI void gemm_body(const u16* __restrict__ X, char* lds, int mbase, int n0,
                  int w, int lane, u16* __restrict__ Qh, u16* __restrict__ Kh,
                  u16* __restrict__ Vt, char* ws, const PrepSrc& S, int b) {
  const int r = lane & 31, hh = lane >> 5;
  f32x16 acc[3];
#pragma unroll
  for (int j = 0; j < 3; ++j)
#pragma unroll
    for (int i = 0; i < 16; ++i) acc[j][i] = 0.f;

  const int g = (lane >> 4) & 1, q = (lane & 15) >> 2, p = lane & 3;
  const unsigned ldsb = (unsigned)(size_t)lds;
  const unsigned wb = ldsb + (8 * hh + q) * 192 + (16 * g + 4 * p) * 2;
  const unsigned xb = ldsb + K1_WBYTES + w * K1_XW;
  const unsigned xo = (unsigned)(hh * 1024 + ((r ^ hh) << 4));
  const unsigned xa = ldsb + K1_WBYTES + w * K1_XW + r * 512;
  const int m0 = mbase + 32 * w;

  bf16x8 af[16];
  asm volatile("s_waitcnt vmcnt(12)" ::: "memory");
  __builtin_amdgcn_s_barrier();
  asm volatile("" ::: "memory");
  STAMP(0, 1);
#pragma unroll
  for (int ks = 0; ks < 16; ++ks) af[ks] = *(const LDS_AS bf16x8*)(xa + (((2 * ks + hh) ^ (r & 15)) * 16));
  asm volatile("s_waitcnt lgkmcnt(0)" ::: "memory");
  k1_issue_x(X, xb, 1, m0, xo);
  asm volatile("" ::: "memory");
  k1_half<NV>(af, wb, acc);
  STAMP(0, 5);
  asm volatile("s_waitcnt vmcnt(0)" ::: "memory");
  __builtin_amdgcn_s_barrier();
  asm volatile("" ::: "memory");
  STAMP(0, 4);
#pragma unroll
  for (int ks = 0; ks < 16; ++ks) af[ks] = *(const LDS_AS bf16x8*)(xa + (((2 * ks + hh) ^ (r & 15)) * 16));
  asm volatile("s_waitcnt lgkmcnt(0)" ::: "memory");
  k1_half<NV>(af, wb + 256 * 192, acc);
  STAMP(0, 2);
#pragma unroll
  for (int j = 0; j < 3; ++j) {
    const int nc = n0 + 32 * j;
    if (j >= 3 - NV) {
      const int nv = nc - 1024 + r;
      const int head = nv >> 4, d = nv & 15;
      u16* dst = Vt + (size_t)(head * 64 + (m0 >> 5)) * 512 + d * 16 + 8 * hh;
      u32x4 w0, w1;
      w0[0] = pk2(acc[j][0], acc[j][1]);  w0[1] = pk2(acc[j][2], acc[j][3]);
      w0[2] = pk2(acc[j][4], acc[j][5]);  w0[3] = pk2(acc[j][6], acc[j][7]);
      w1[0] = pk2(acc[j][8], acc[j][9]);  w1[1] = pk2(acc[j][10], acc[j][11]);
      w1[2] = pk2(acc[j][12], acc[j][13]); w1[3] = pk2(acc[j][14], acc[j][15]);
      st16(dst, w0);
      st16(dst + 256, w1);
    } else {
      u16* base = (nc < 512) ? Qh : Kh;
      const int head0 = (nc & 511) >> 4;
#pragma unroll
      for (int e = 0; e < 2; ++e) {
        unsigned a0 = pk2(acc[j][8 * e], acc[j][8 * e + 1]), a1 = pk2(acc[j][8 * e + 2], acc[j][8 * e + 3]);
        unsigned b0 = pk2(acc[j][8 * e + 4], acc[j][8 * e + 5]), b1 = pk2(acc[j][8 * e + 6], acc[j][8 * e + 7]);
        auto r0 = __builtin_amdgcn_permlane32_swap(a0, b0, false, false);
        auto r1 = __builtin_amdgcn_permlane32_swap(a1, b1, false, false);
        u32x4 wv = {r0[0], r1[0], r0[1], r1[1]};
        u16* dst = base + ((size_t)(head0 + e) * SEQ + m0 + r) * DH + 8 * hh;
        st16(dst, wv);
      }
    }
  }
}

#define GLD1(dst, voff, sb, imm) asm volatile("global_load_dword %0, %1, %2 offset:" #imm : "=v"(dst) : "v"(voff), "s"(uni(sb)))
#define GLD4(dst, voff, sb, imm) asm volatile("global_load_dwordx4 %0, %1, %2 offset:" #imm : "=v"(dst) : "v"(voff), "s"(uni(sb)))
#define GLDH(dst, voff, sb, imm) asm volatile("global_load_ushort %0, %1, %2 offset:" #imm : "=v"(dst) : "v"(voff), "s"(uni(sb)))
DI void prep_load(PrepIn& P, const u16* __restrict__ Aq, const u16* __restrict__ Ak, const u16* __restrict__ Av,
                  const u16* __restrict__ shiftk, const u16* __restrict__ wo, const u16* __restrict__ gatew,
                  int b, int pw, int lane) {
  const int st = b >> 3, dt = b & 7;
  const int dg = lane & 7, sg = lane >> 3;
  const int d0 = dt * 64 + dg * 8 + 2 * pw;
  const int s0 = st * 64 + sg * 8;
  const unsigned va = (unsigned)((s0 == 0 ? 0 : s0 - 3) * 1024 + d0 * 2);
  const unsigned vb = (unsigned)(s0 * 1024 + d0 * 2);
  const char* k2 = (const char*)Ak + 4096;
  const char* v2 = (const char*)Av + 4096;
  GLD1(P.kk[0], va, Ak, 0); GLD1(P.kk[1], va, Ak, 1024); GLD1(P.kk[2], va, Ak, 2048);
  GLD1(P.kk[3], vb, Ak, 0); GLD1(P.kk[4], vb, Ak, 1024); GLD1(P.kk[5], vb, Ak, 2048); GLD1(P.kk[6], vb, Ak, 3072);
  GLD1(P.kk[7], vb, k2, 0); GLD1(P.kk[8], vb, k2, 1024); GLD1(P.kk[9], vb, k2, 2048); GLD1(P.kk[10], vb, k2, 3072);
  GLD1(P.vv[0], vb, Av, 0); GLD1(P.vv[1], vb, Av, 1024); GLD1(P.vv[2], vb, Av, 2048); GLD1(P.vv[3], vb, Av, 3072);
  GLD1(P.vv[4], vb, v2, 0); GLD1(P.vv[5], vb, v2, 1024); GLD1(P.vv[6], vb, v2, 2048); GLD1(P.vv[7], vb, v2, 3072);
  const unsigned vs = (unsigned)(d0 * 8), vl = (unsigned)(lane * 16), vh = (unsigned)(lane * 2);
  GLD4(P.skw, vs, shiftk, 0);
  GLD4(P.gw, vl, gatew, 0);
  const char* gq = (const char*)Aq + (size_t)(b * 8 + pw * 2) * 1024;
  GLD4(P.gx[0], vl, gq, 0); GLD4(P.gx[1], vl, gq, 1024);
  const char* wr = (const char*)wo + (size_t)((b >> 3) * 16 + pw * 4) * 1024 + (b & 7) * 128;
  GLDH(P.wv[0], vh, wr, 0); GLDH(P.wv[1], vh, wr, 1024); GLDH(P.wv[2], vh, wr, 2048); GLDH(P.wv[3], vh, wr, 3072);
}
DI void prep_wait(PrepIn& P) {
  __builtin_amdgcn_sched_barrier(0);
  asm volatile("s_waitcnt vmcnt(0)" ::: "memory");
  __builtin_amdgcn_sched_barrier(0);
  asm volatile(""
               : "+v"(P.kk[0]), "+v"(P.kk[1]), "+v"(P.kk[2]), "+v"(P.kk[3]), "+v"(P.kk[4]), "+v"(P.kk[5]),
                 "+v"(P.kk[6]), "+v"(P.kk[7]), "+v"(P.kk[8]), "+v"(P.kk[9]), "+v"(P.kk[10]),
                 "+v"(P.vv[0]), "+v"(P.vv[1]), "+v"(P.vv[2]), "+v"(P.vv[3]), "+v"(P.vv[4]), "+v"(P.vv[5]),
                 "+v"(P.vv[6]), "+v"(P.vv[7]));
  asm volatile("" : "+v"(P.skw), "+v"(P.gw), "+v"(P.gx[0]), "+v"(P.gx[1]),
                    "+v"(P.wv[0]), "+v"(P.wv[1]), "+v"(P.wv[2]), "+v"(P.wv[3]));
}

DI void prep_compute(PrepIn& P, PrepOut& O, int b, int lane) {
  const int s0 = (b >> 3) * 64 + (lane >> 3) * 8;
  if (s0 == 0) { P.kk[0] = 0; P.kk[1] = 0; P.kk[2] = 0; }
#pragma unroll
  for (int c = 0; c < 2; ++c) {
    const unsigned s01 = P.skw[c * 2], s23 = P.skw[c * 2 + 1];
    const float sk0 = bflo(s01), sk1 = bfhi(s01), sk2 = bflo(s23), sk3 = bfhi(s23);
    float kf[11];
#pragma unroll
    for (int i = 0; i < 11; ++i) kf[i] = c ? bfhi(P.kk[i]) : bflo(P.kk[i]);
    float kvv[8];
#pragma unroll
    for (int i = 0; i < 8; ++i) {
      float acc = rbf(kf[i] * sk3);
      acc = rbf(acc + rbf(kf[i + 1] * sk2));
      acc = rbf(acc + rbf(kf[i + 2] * sk1));
      acc = rbf(acc + rbf(kf[i + 3] * sk0));
      const float vf = c ? bfhi(P.vv[i]) : bflo(P.vv[i]);
      kvv[i] = acc * vf;
    }
    O.kv[c][0] = pk2(kvv[0], kvv[1]); O.kv[c][1] = pk2(kvv[2], kvv[3]);
    O.kv[c][2] = pk2(kvv[4], kvv[5]); O.kv[c][3] = pk2(kvv[6], kvv[7]);
  }
  O.wo[0] = P.wv[0] | (P.wv[1] << 16); O.wo[1] = P.wv[2] | (P.wv[3] << 16);
#pragma unroll
  for (int i = 0; i < 2; ++i) {
    float sum = 0.f;
#pragma unroll
    for (int e = 0; e < 4; ++e) {
      sum += rbf(bflo(P.gx[i][e]) * bflo(P.gw[e]));
      sum += rbf(bfhi(P.gx[i][e]) * bfhi(P.gw[e]));
    }
#pragma unroll
    for (int o = 32; o > 0; o >>= 1) sum += __shfl_xor(sum, o);
    const float z = rbf(sum);
    O.gv[i] = rbf(1.f / (1.f + __expf(-z)));
  }
}

DI void prep_store(const PrepOut& O, char* __restrict__ ws, int b, int pw, int lane) {
  const int st = b >> 3, dt = b & 7;
  const int dg = lane & 7, sg = lane >> 3;
  const int d0 = dt * 64 + dg * 8 + 2 * pw;
  const int s0 = st * 64 + sg * 8;
  u16* kvT = (u16*)(ws + WS_KVT);
  st16(kvT + (size_t)d0 * SEQ + s0, O.kv[0]);
  st16(kvT + (size_t)(d0 + 1) * SEQ + s0, O.kv[1]);
  const int wk0 = (b >> 3) * 16 + pw * 4, wn = (b & 7) * 64 + lane;
  u16* woT = (u16*)(ws + WS_WOT);
  const size_t off = ((size_t)(((wn >> 5) * 32 + (wk0 >> 4)) * 2 + ((wk0 >> 3) & 1)) * 32 + (wn & 31)) * 8 + (wk0 & 7);
  st8(woT + off, O.wo);
  float* gout = (float*)(ws + WS_G);
  if (lane < 2) gout[b * 8 + pw * 2 + lane] = lane ? O.gv[1] : O.gv[0];
}

__global__ __launch_bounds__(512, 2) void k1_gemm_prep(
    const u16* __restrict__ Aq, const u16* __restrict__ Ak, const u16* __restrict__ Av,
    const u16* __restrict__ shiftk, const u16* __restrict__ wqkv, const u16* __restrict__ wo,
    const u16* __restrict__ gatew, char* __restrict__ ws) {
  __shared__ __attribute__((aligned(16))) char lds[K1_WBYTES + 4 * K1_XW];
  const int tid = threadIdx.x, lane = tid & 63;
  const int w = __builtin_amdgcn_readfirstlane(tid >> 6);
  const int b = blockIdx.x;
  STAMP(0, 0);
  if (w >= 4) {
    __builtin_amdgcn_s_setprio(3);
    PrepIn P;
    prep_load(P, Aq, Ak, Av, shiftk, wo, gatew, b, w - 4, lane);
    __builtin_amdgcn_s_barrier();
    prep_wait(P);
    PrepOut O;
    prep_compute(P, O, b, lane);
    prep_store(O, ws, b, w - 4, lane);
    return;
  }
  const int xcd = b & 7, idx = b >> 3;
  const int mt = (xcd >> 1) * 4 + (idx >> 3);
  const int nt = (xcd & 1) * 8 + (idx & 7);
  const int n0 = nt * 96, mbase = mt * 128;
  const int rot = (idx >> 3) * 4;
  const unsigned ldsb = (unsigned)(size_t)lds;
  unsigned wo3[3];
#pragma unroll
  for (int j = 0; j < 3; ++j) {
    const int ci = j * 64 + lane, row = ci / 12, c16 = ci - row * 12;
    wo3[j] = (unsigned)(row * 3072 + c16 * 16);
  }
  const unsigned xo = (unsigned)((lane >> 5) * 1024 + (((lane & 31) ^ (lane >> 5)) << 4));

  const PrepSrc P = {Ak, Av, shiftk, wo, gatew};
  k1_issue_w(wqkv, ldsb, 0, n0, w, wo3, rot);
  k1_issue_x(Aq, ldsb + K1_WBYTES + w * K1_XW, 0, mbase + 32 * w, xo);
  k1_issue_w(wqkv, ldsb, 1, n0, w, wo3, rot);

  u16* Qh = (u16*)(ws + WS_Q);
  u16* Kh = (u16*)(ws + WS_K);
  u16* Vt = (u16*)(ws + WS_VT);
  if (nt <= 9) gemm_body<0>(Aq, lds, mbase, n0, w, lane, Qh, Kh, Vt, ws, P, b);
  else if (nt == 10) gemm_body<1>(Aq, lds, mbase, n0, w, lane, Qh, Kh, Vt, ws, P, b);
  else gemm_body<3>(Aq, lds, mbase, n0, w, lane, Qh, Kh, Vt, ws, P, b);
  STAMP(0, 3);
}

#define CSTR 4608
#define XR_OFF (8 * CSTR)
#define XR_STR 80
#define K2_LDS (XR_OFF + 95 * XR_STR)

struct ConvRegs { u32x4 h0, h1, hx0, hx1, xv; };

DI void conv_load(ConvRegs& R, int d, const u16* __restrict__ ssmk, char* __restrict__ ws) {
  const int tid = threadIdx.x, lane = tid & 63, w = tid >> 6;
  const int r = lane & 31, hh = lane >> 5;
  const u16* hrow = ssmk + (size_t)d * SEQ;
  const u32x4 z4 = {0u, 0u, 0u, 0u};
  R.h0 = *(const u32x4*)(hrow + 8 * (tid >= 4 ? tid - 4 : 0));
  R.h1 = *(const u32x4*)(hrow + 8 * (tid >= 3 ? tid - 3 : 0));
  if (tid < 4) R.h0 = z4;
  if (tid < 3) R.h1 = z4;
  R.hx0 = z4; R.hx1 = z4;
  if (tid < 4) {
    R.hx0 = *(const u32x4*)(hrow + 8 * (252 + tid));
    if (tid < 3) R.hx1 = *(const u32x4*)(hrow + 8 * (253 + tid));
  }
  R.xv = *(const u32x4*)((const u16*)(ws + WS_KVT) + (size_t)d * SEQ + 8 * tid);
}

DI void conv_write_copies(char* lds, int t, u32x4 h0, u32x4 h1) {
  unsigned wv[8] = {h0[0], h0[1], h0[2], h0[3], h1[0], h1[1], h1[2], h1[3]};
  unsigned od[7];
#pragma unroll
  for (int i = 0; i < 7; ++i) od[i] = __builtin_amdgcn_alignbyte(wv[i + 1], wv[i], 2);
#pragma unroll
  for (int c = 0; c < 8; ++c) {
    const int e = c >> 1;
    u32x4 v;
    if (c & 1) { v[0] = od[e]; v[1] = od[e + 1]; v[2] = od[e + 2]; v[3] = od[e + 3]; }
    else       { v[0] = wv[e]; v[1] = wv[e + 1]; v[2] = wv[e + 2]; v[3] = wv[e + 3]; }
    const unsigned off = (0x5D9159D0u >> (4 * c)) & 15u;
    *(u32x4*)(lds + c * CSTR + off * 16 + t * 16) = v;
  }
}

DI void conv_compute(const ConvRegs& R, int d, char* lds, const u16* __restrict__ ssmD,
                     char* __restrict__ ws) {
  const int tid = threadIdx.x, lane = tid & 63, w = tid >> 6;
  const int r = lane & 31, hh = lane >> 5;
  {
    const u32x4 z4 = {0u, 0u, 0u, 0u};
    conv_write_copies(lds, tid, R.h0, R.h1);
    if (tid < 4) conv_write_copies(lds, 256 + tid, R.hx0, R.hx1);
    u32x4 rv;
#pragma unroll
    for (int i = 0; i < 4; ++i) rv[i] = __builtin_amdgcn_alignbit(R.xv[3 - i], R.xv[3 - i], 16);
    *(u32x4*)(lds + XR_OFF + ((tid >> 2) + 31) * XR_STR + (3 - (tid & 3)) * 16) = rv;
    if (tid < 124) *(u32x4*)(lds + XR_OFF + (tid >> 2) * XR_STR + (tid & 3) * 16) = z4;
  }
  __syncthreads();
  if (!(d & 1)) STAMP(1, 1); else STAMP(1, 4);

  f32x16 acc0, acc1a, acc1b;
#pragma unroll
  for (int i = 0; i < 16; ++i) { acc0[i] = 0.f; acc1a[i] = 0.f; acc1b[i] = 0.f; }
  {
    const int c = (1 + r) & 7, u = (1 + r) >> 3;
    const unsigned off = (0x5D9159D0u >> (4 * c)) & 15u;
    const char* abase = lds + c * CSTR + off * 16 + (16 + 8 * hh + 8 * u) * 2 + w * 8 * 64;
    const char* bbase = lds + XR_OFF + (r + 31 - w * 8) * XR_STR + 16 * hh;
#pragma unroll
    for (int gi = 0; gi < 8; ++gi) {
#pragma unroll
      for (int ks = 0; ks < 2; ++ks) {
        const bf16x8 A0 = *(const bf16x8*)(abase + gi * 64 - ks * 32);
        const bf16x8 A1 = *(const bf16x8*)(abase + gi * 64 - ks * 32 + 32 * 64);
        const bf16x8 B0 = *(const bf16x8*)(bbase - gi * XR_STR + (1 - ks) * 32);
        const bf16x8 B1 = *(const bf16x8*)(bbase + (32 - gi) * XR_STR + (1 - ks) * 32);
        acc0 = MFMA(A0, B0, acc0);
        acc1a = MFMA(A1, B0, acc1a);
        acc1b = MFMA(A0, B1, acc1b);
      }
    }
  }
  __syncthreads();
  if (!(d & 1)) STAMP(1, 5); else STAMP(1, 6);
  {
    float* red = (float*)lds;
#pragma unroll
    for (int dst = 0; dst < 4; ++dst)
#pragma unroll
      for (int hf = 0; hf < 2; ++hf) {
        f32x4 v;
#pragma unroll
        for (int i = 0; i < 4; ++i) {
          const int rg = 8 * (dst & 1) + 4 * hf + i;
          v[i] = (dst >> 1) ? (acc1a[rg] + acc1b[rg]) : acc0[rg];
        }
        *(f32x4*)(red + ((((w * 4 + dst) * 2 + hf) * 64) + lane) * 4) = v;
      }
  }
  __syncthreads();
  {
    const float* red = (const float*)lds;
    const float Dv = bf2f(ssmD[d]);
    u16* aT = (u16*)(ws + WS_AT) + (size_t)d * SEQ;
    const int ablk = 32 * (w >> 1) + r;
#pragma unroll
    for (int hf = 0; hf < 2; ++hf) {
      f32x4 y = {0.f, 0.f, 0.f, 0.f};
#pragma unroll
      for (int src = 0; src < 4; ++src) {
        const f32x4 v = *(const f32x4*)(red + ((((src * 4 + w) * 2 + hf) * 64) + lane) * 4);
        y += v;
      }
      const int b0 = 16 * (w & 1) + 8 * hf + 4 * hh;
      const int s = 32 * ablk + b0;
      const u32x2 xv = *(const u32x2*)(lds + XR_OFF + (ablk + 31) * XR_STR + (28 - b0) * 2);
      float kv[4] = {bfhi(xv[1]), bflo(xv[1]), bfhi(xv[0]), bflo(xv[0])};
      float a[4];
#pragma unroll
      for (int i = 0; i < 4; ++i) {
        a[i] = rbf(y[i]) + rbf(kv[i] * Dv);
      }
      u32x2 o;
      o[0] = pk2(a[0], a[1]); o[1] = pk2(a[2], a[3]);
      st8(aT + s, o);
    }
  }
  __syncthreads();
}

DI void attn_task(const char* lds, int lt0, int qb, bf16x8 qf, int h, char* __restrict__ ws) {
  const int lane = threadIdx.x & 63;
  const int c = lane & 31, hh = lane >> 5;
  f32x16 zero;
#pragma unroll
  for (int i = 0; i < 16; ++i) zero[i] = 0.f;
  f32x16 sc[9];
  const char* kb = lds + lt0 * 1024 + c * 32 + hh * 16;
#pragma unroll
  for (int t = 0; t < 9; ++t) {
    const bf16x8 kf = *(const bf16x8*)(kb + t * 1024);
    sc[t] = MFMA(kf, qf, zero);
  }
  const float NEG = -1e30f;
#pragma unroll
  for (int i = 0; i < 16; ++i) {
    const int row = (i & 3) + 8 * (i >> 2) + 4 * hh;
    sc[0][i] = (row > c) ? sc[0][i] : NEG;
    sc[8][i] = (row <= c) ? sc[8][i] : NEG;
  }
  if (qb < 8) {
#pragma unroll
    for (int t = 0; t < 8; ++t)
      if (qb - 8 + t < 0) {
#pragma unroll
        for (int i = 0; i < 16; ++i) sc[t][i] = NEG;
      }
  }
  float m = NEG;
#pragma unroll
  for (int t = 0; t < 9; ++t)
#pragma unroll
    for (int i = 0; i < 16; ++i) m = fmaxf(m, sc[t][i]);
  m = fmaxf(m, __shfl_xor(m, 32));
  const float cs = 0.25f * 1.4426950408889634f;
  const float mc = m * cs;
  f32x16 oacc[2];
  oacc[0] = zero; oacc[1] = zero;
  const unsigned onesw = (c == 16 || c == 20) ? 0x3F803F80u : 0u;
  const char* vb = lds + 16384 + lt0 * 1024 + (c & 15) * 32 + hh * 16;
#pragma unroll
  for (int t = 0; t < 9; ++t) {
#pragma unroll
    for (int s = 0; s < 2; ++s) {
      u32x4 vw = *(const u32x4*)(vb + t * 1024 + s * 512);
      if (c >= 16) { vw[0] = onesw; vw[1] = onesw; vw[2] = onesw; vw[3] = onesw; }
      u32x4 pw;
#pragma unroll
      for (int e = 0; e < 4; ++e) {
        const float p0 = __builtin_amdgcn_exp2f(fmaf(sc[t][8 * s + 2 * e], cs, -mc));
        const float p1 = __builtin_amdgcn_exp2f(fmaf(sc[t][8 * s + 2 * e + 1], cs, -mc));
        pw[e] = pk2(p0, p1);
      }
      oacc[s] = MFMA(__builtin_bit_cast(bf16x8, vw), __builtin_bit_cast(bf16x8, pw), oacc[s]);
    }
  }
  const float inv = 1.f / (oacc[0][8] + oacc[1][8]);
  unsigned a0 = pk2((oacc[0][0] + oacc[1][0]) * inv, (oacc[0][1] + oacc[1][1]) * inv);
  unsigned a1 = pk2((oacc[0][2] + oacc[1][2]) * inv, (oacc[0][3] + oacc[1][3]) * inv);
  unsigned b0 = pk2((oacc[0][4] + oacc[1][4]) * inv, (oacc[0][5] + oacc[1][5]) * inv);
  unsigned b1 = pk2((oacc[0][6] + oacc[1][6]) * inv, (oacc[0][7] + oacc[1][7]) * inv);
  auto r0 = __builtin_amdgcn_permlane32_swap(a0, b0, false, false);
  auto r1 = __builtin_amdgcn_permlane32_swap(a1, b1, false, false);
  u32x4 wv = {r0[0], r1[0], r0[1], r1[1]};
  u16* o = (u16*)(ws + WS_O) + ((size_t)((qb * 32 + h) * 2 + hh) * 32 + c) * 8;
  st16(o, wv);
}

DI void attn_body(int h, int g, char* lds, char* __restrict__ ws) {
  const int tid = threadIdx.x, lane = tid & 63;
  const int w = __builtin_amdgcn_readfirstlane(tid >> 6);
  const int c = lane & 31, hh = lane >> 5;
  const u16* Qh = (const u16*)(ws + WS_Q) + (size_t)h * SEQ * DH;
  const u16* Kh = (const u16*)(ws + WS_K) + (size_t)h * SEQ * DH;
  const u16* Vt = (const u16*)(ws + WS_VT) + (size_t)h * 64 * 512;
#pragma unroll
  for (int i = 0; i < 4; ++i) {
    const int lt = w * 4 + i;
    int gt = 8 * g - 8 + lt; gt = gt < 0 ? 0 : gt;
    __builtin_amdgcn_global_load_lds((GLB_AS const void*)(Kh + (size_t)gt * 512 + lane * 8),
                                     (LDS_AS void*)(lds + lt * 1024), 16, 0, 0);
    __builtin_amdgcn_global_load_lds((GLB_AS const void*)(Vt + (size_t)gt * 512 + lane * 8),
                                     (LDS_AS void*)(lds + 16384 + lt * 1024), 16, 0, 0);
  }
  const int qb0 = 8 * g + w, qb1 = 8 * g + w + 4;
  const bf16x8 q0 = *(const bf16x8*)(Qh + (size_t)(32 * qb0 + c) * DH + 8 * hh);
  const bf16x8 q1 = *(const bf16x8*)(Qh + (size_t)(32 * qb1 + c) * DH + 8 * hh);
  asm volatile("s_waitcnt vmcnt(0)" ::: "memory");
  __syncthreads();
  STAMP(1, 1);
  attn_task(lds, w, qb0, q0, h, ws);
  STAMP(1, 2);
  attn_task(lds, w + 4, qb1, q1, h, ws);
}

__global__ __launch_bounds__(256, 2) void k2_attn_conv(const u16* __restrict__ ssmk,
                                                       const u16* __restrict__ ssmD,
                                                       char* __restrict__ ws) {
  __shared__ __attribute__((aligned(16))) char lds[K2_LDS];
  const int b = blockIdx.x;
  STAMP(1, 0);
  if (b >= 256) {
    const int a = b - 256;
    attn_body(a >> 3, a & 7, lds, ws);
  } else {
    ConvRegs R0, R1;
    conv_load(R0, 2 * b, ssmk, ws);
    conv_load(R1, 2 * b + 1, ssmk, ws);
    conv_compute(R0, 2 * b, lds, ssmD, ws);
    STAMP(1, 2);
    conv_compute(R1, 2 * b + 1, lds, ssmD, ws);
  }
  STAMP(1, 3);
}

__global__ __launch_bounds__(256, 1) void k3_outproj(char* __restrict__ ws, const u16* __restrict__ Aq, u16* __restrict__ out) {
  __shared__ __attribute__((aligned(16))) char lds[131072];
  const int tid = threadIdx.x, lane = tid & 63;
  const int w = __builtin_amdgcn_readfirstlane(tid >> 6);
  const int c = lane & 31, hh = lane >> 5;
  const int b = blockIdx.x;
  STAMP(2, 0);
  const int st = (b & 7) * 4 + (b >> 6), ntl = (b >> 3) & 7;
  {
    const u16* src = (w < 2) ? (const u16*)(ws + WS_WOT) + (size_t)(ntl * 2 + w) * 16384
                             : (const u16*)(ws + WS_O) + (size_t)(st * 2 + w - 2) * 16384;
    src += lane * 8;
#pragma unroll
    for (int i = 0; i < 32; ++i)
      __builtin_amdgcn_global_load_lds((GLB_AS const void*)(src + i * 512), (LDS_AS void*)(lds + w * 32768 + i * 1024), 16, 0, 0);
  }
  asm volatile("" ::: "memory");
  const int ntile = ntl * 2 + (w & 1), stile = st * 2 + (w >> 1);
  const int n0 = ntile * 32, s0 = stile * 32;
  const int s = s0 + c;
  const float g = ((const float*)(ws + WS_G))[s];
  const u16* aT = (const u16*)(ws + WS_AT);
  unsigned av[16];
#pragma unroll
  for (int i = 0; i < 16; ++i) av[i] = aT[(size_t)(n0 + (i & 3) + 8 * (i >> 2) + 4 * hh) * SEQ + s];
  u32x2 qv[4];
#pragma unroll
  for (int gq = 0; gq < 4; ++gq) qv[gq] = *(const u32x2*)(Aq + (size_t)s * DM + n0 + 8 * gq + 4 * hh);
  asm volatile("" ::: "memory");
  f32x16 acc;
#pragma unroll
  for (int i = 0; i < 16; ++i) acc[i] = 0.f;
  const char* ab = lds + (w & 1) * 32768 + lane * 16;
  const char* bb = lds + (2 + (w >> 1)) * 32768 + lane * 16;
#pragma unroll
  for (int ch = 0; ch < 4; ++ch) {
    if (ch == 0) asm volatile("s_waitcnt vmcnt(45)" ::: "memory");
    if (ch == 1) asm volatile("s_waitcnt vmcnt(37)" ::: "memory");
    if (ch == 2) asm volatile("s_waitcnt vmcnt(29)" ::: "memory");
    if (ch == 3) asm volatile("s_waitcnt vmcnt(21)" ::: "memory");
    __builtin_amdgcn_s_barrier();
    asm volatile("" ::: "memory");
    bf16x8 af[8], bf[8];
#pragma unroll
    for (int ks = 0; ks < 8; ++ks) {
      af[ks] = *(const bf16x8*)(ab + (ch * 8 + ks) * 1024);
      bf[ks] = *(const bf16x8*)(bb + (ch * 8 + ks) * 1024);
    }
    __builtin_amdgcn_sched_barrier(0);
#pragma unroll
    for (int ks = 0; ks < 8; ++ks) acc = MFMA(af[ks], bf[ks], acc);
    __builtin_amdgcn_sched_barrier(0);
  }
  STAMP(2, 2);
  const float omg = rbf(1.f - g);
#pragma unroll
  for (int gq = 0; gq < 4; ++gq) {
    const int n = n0 + 8 * gq + 4 * hh;
    float res[4];
#pragma unroll
    for (int i = 0; i < 4; ++i) {
      const float qf = (i & 1) ? bfhi(qv[gq][i >> 1]) : bflo(qv[gq][i >> 1]);
      const float a = rbf(qf * bf2f(av[4 * gq + i]));
      const float bb2 = rbf(acc[4 * gq + i]);
      res[i] = rbf(g * a) + rbf(omg * bb2);
    }
    u32x2 wv;
    wv[0] = pk2(res[0], res[1]); wv[1] = pk2(res[2], res[3]);
    st8(out + (size_t)s * DM + n, wv);
  }
  STAMP(2, 3);
}

extern "C" void kernel_launch(void* const* d_in, const int* in_sizes, int n_in,
                              void* d_out, int out_size, void* d_ws, size_t ws_size,
                              hipStream_t stream) {
  const u16* Aq = (const u16*)d_in[0];
  const u16* Ak = (const u16*)d_in[1];
  const u16* Av = (const u16*)d_in[2];
  const u16* shiftk = (const u16*)d_in[3];
  const u16* ssmk = (const u16*)d_in[4];
  const u16* ssmD = (const u16*)d_in[5];
  const u16* wqkv = (const u16*)d_in[7];
  const u16* wo = (const u16*)d_in[8];
  const u16* gatew = (const u16*)d_in[9];
  char* ws = (char*)d_ws;
  k1_gemm_prep<<<256, 512, 0, stream>>>(Aq, Ak, Av, shiftk, wqkv, wo, gatew, ws);
  k2_attn_conv<<<512, 256, 0, stream>>>(ssmk, ssmD, ws);
  k3_outproj<<<256, 256, 0, stream>>>(ws, Aq, (u16*)d_out);
}
```

```cpp
#include <hip/hip_runtime.h>
#include <hip/hip_bf16.h>
#include <stdint.h>

#define SEQ 2048
#define DM 512
#define NH 32
#define DH 16

typedef __attribute__((ext_vector_type(8))) short bf16x8;
typedef __attribute__((ext_vector_type(4))) short s16x4;
typedef __attribute__((ext_vector_type(16))) float f32x16;
typedef __attribute__((ext_vector_type(4))) float f32x4;
typedef __attribute__((ext_vector_type(4))) unsigned u32x4;
typedef __attribute__((ext_vector_type(2))) unsigned u32x2;
typedef __attribute__((ext_vector_type(2))) __bf16 bf16x2_t;
typedef unsigned short u16;

#define DI __device__ __forceinline__
#define MFMA(a, b, c) __builtin_amdgcn_mfma_f32_32x32x16_bf16((a), (b), (c), 0, 0, 0)
#define LDS_AS __attribute__((address_space(3)))
#define GLB_AS __attribute__((address_space(1)))

DI float bf2f(unsigned v16) { return __uint_as_float(v16 << 16); }
DI float bflo(unsigned w) { return __uint_as_float(w << 16); }
DI float bfhi(unsigned w) { return __uint_as_float(w & 0xffff0000u); }
DI unsigned pk2(float lo, float hi) {
  bf16x2_t v; v[0] = (__bf16)lo; v[1] = (__bf16)hi;
  return __builtin_bit_cast(unsigned, v);
}
DI float rbf(float x) {
  bf16x2_t v; v[0] = (__bf16)x; v[1] = (__bf16)0.f;
  return __uint_as_float(__builtin_bit_cast(unsigned, v) << 16);
}

#define ST_MOD "sc1"
DI void st16(void* p, u32x4 v) {
  asm volatile("global_store_dwordx4 %0, %1, off " ST_MOD "\n\ts_nop 1" :: "v"(p), "v"(v) : "memory");
}
DI void st8(void* p, u32x2 v) {
  asm volatile("global_store_dwordx2 %0, %1, off\n\ts_nop 1" :: "v"(p), "v"(v) : "memory");
}
#define STAMP(kid, slot)
#define WS_Q   (0u)
#define WS_K   (2u << 20)
#define WS_VT  (4u << 20)
#define WS_KVT (6u << 20)
#define WS_QT  (8u << 20)
#define WS_AT  (10u << 20)
#define WS_O   (12u << 20)
#define WS_WOT (14u << 20)
#define WS_G   (15u << 20)

#define K1_WBYTES (512 * 192)
#define K1_XW 16384

DI const char* uni(const void* p) {
  const uint64_t v = (uint64_t)p;
  const unsigned lo = __builtin_amdgcn_readfirstlane((unsigned)v), hi = __builtin_amdgcn_readfirstlane((unsigned)(v >> 32));
  return (const char*)(((uint64_t)hi << 32) | lo);
}
DI void glds16(unsigned voff, const void* sbase, unsigned ldsaddr) {
  asm volatile("s_mov_b32 m0, %2\n\ts_nop 0\n\tglobal_load_lds_dwordx4 %0, %1"
               :: "v"(voff), "s"(uni(sbase)), "s"(__builtin_amdgcn_readfirstlane(ldsaddr)) : "memory");
}
DI void k1_issue_w(const u16* __restrict__ W, unsigned ldsb, int half, int n0, int w, const unsigned (&wo3)[3], int rot) {
#pragma unroll
  for (int i = 0; i < 4; ++i) {
    const int g = half * 16 + ((4 * i + w + rot) & 15);
    const char* sb = (const char*)W + (size_t)g * (16 * 3072) + n0 * 2;
#pragma unroll
    for (int j = 0; j < 3; ++j) glds16(wo3[j], sb, ldsb + g * 3072 + j * 1024);
  }
}
DI void k1_issue_x(const u16* __restrict__ X, unsigned xb, int half, int m0, unsigned xo) {
#pragma unroll
  for (int i = 0; i < 16; ++i) {
    const char* sb = (const char*)X + (size_t)(m0 + 2 * i) * 1024 + half * 512;
    glds16(xo ^ (unsigned)(((2 * i) & 15) << 4), sb, xb + i * 1024);
  }
}

template <int OFF>
DI void tr_rd(s16x4& d, unsigned base) {
  asm volatile("ds_read_b64_tr_b16 %0, %1 offset:%2" : "=v"(d) : "v"(base), "i"(OFF));
}
template <int KS>
DI void k1_rdb(s16x4 (&t)[6], unsigned base) {
  tr_rd<KS * 3072 + 0>(t[0], base);   tr_rd<KS * 3072 + 768>(t[1], base);
  tr_rd<KS * 3072 + 64>(t[2], base);  tr_rd<KS * 3072 + 64 + 768>(t[3], base);
  tr_rd<KS * 3072 + 128>(t[4], base); tr_rd<KS * 3072 + 128 + 768>(t[5], base);
}
template <int N>
DI void lgkm_wait6(s16x4 (&t)[6]) {
  asm volatile("s_waitcnt lgkmcnt(%6)" : "+v"(t[0]), "+v"(t[1]), "+v"(t[2]), "+v"(t[3]), "+v"(t[4]), "+v"(t[5]) : "i"(N));
}
template <int NV, int KS>
DI void k1_steps(const bf16x8 (&af)[16], unsigned wb, f32x16 (&acc)[3], s16x4 (&t0)[6], s16x4 (&t1)[6], s16x4 (&t2)[6]) {
  if constexpr (KS + 1 < 16) lgkm_wait6<6>(t0); else lgkm_wait6<0>(t0);
#pragma unroll
  for (int j = 0; j < 3; ++j) {
    const bf16x8 bfr = __builtin_shufflevector(t0[2 * j], t0[2 * j + 1], 0, 1, 2, 3, 4, 5, 6, 7);
    if (j >= 3 - NV) acc[j] = MFMA(af[KS], bfr, acc[j]);
    else             acc[j] = MFMA(bfr, af[KS], acc[j]);
    if (j == 0) { if constexpr (KS + 2 < 16) k1_rdb<KS + 2>(t2, wb); }
  }
  __builtin_amdgcn_sched_barrier(0);
  if constexpr (KS + 1 < 16) k1_steps<NV, KS + 1>(af, wb, acc, t1, t2, t0);
}
template <int NV>
DI void k1_half(const bf16x8 (&af)[16], unsigned wb, f32x16 (&acc)[3]) {
  s16x4 t0[6], t1[6], t2[6];
  k1_rdb<0>(t0, wb);
  k1_rdb<1>(t1, wb);
  k1_steps<NV, 0>(af, wb, acc, t0, t1, t2);
}

struct EpiCtx { int n0, m0, r, hh; u16 *Qh, *Kh, *Vt; };
template <int NV, int J>
DI void k1_epi(const f32x16& a, const EpiCtx& E) {
  const int nc = E.n0 + 32 * J;
  if constexpr (J >= 3 - NV) {
    const int nv = nc - 1024 + E.r;
    const int head = nv >> 4, d = nv & 15;
    u16* dst = E.Vt + (size_t)(head * 64 + (E.m0 >> 5)) * 512 + d * 16 + 8 * E.hh;
    u32x4 w0, w1;
    w0[0] = pk2(a[0], a[1]);  w0[1] = pk2(a[2], a[3]);
    w0[2] = pk2(a[4], a[5]);  w0[3] = pk2(a[6], a[7]);
    w1[0] = pk2(a[8], a[9]);  w1[1] = pk2(a[10], a[11]);
    w1[2] = pk2(a[12], a[13]); w1[3] = pk2(a[14], a[15]);
    st16(dst, w0);
    st16(dst + 256, w1);
  } else {
    u16* base = (nc < 512) ? E.Qh : E.Kh;
    const int head0 = (nc & 511) >> 4;
#pragma unroll
    for (int e = 0; e < 2; ++e) {
      unsigned a0 = pk2(a[8 * e], a[8 * e + 1]), a1 = pk2(a[8 * e + 2], a[8 * e + 3]);
      unsigned b0 = pk2(a[8 * e + 4], a[8 * e + 5]), b1 = pk2(a[8 * e + 6], a[8 * e + 7]);
      auto r0 = __builtin_amdgcn_permlane32_swap(a0, b0, false, false);
      auto r1 = __builtin_amdgcn_permlane32_swap(a1, b1, false, false);
      u32x4 wv = {r0[0], r1[0], r0[1], r1[1]};
      u16* dst = base + ((size_t)(head0 + e) * SEQ + E.m0 + E.r) * DH + 8 * E.hh;
      st16(dst, wv);
    }
  }
}
template <int I>
DI void k1_rd1(s16x4 (&t)[2], unsigned base) {
  tr_rd<(I % 16) * 3072 + (I / 16) * 64>(t[0], base);
  tr_rd<(I % 16) * 3072 + (I / 16) * 64 + 768>(t[1], base);
}
template <int NV, int I>
DI void k1_jsteps(const bf16x8 (&af)[16], unsigned wb, f32x16 (&acc)[3], s16x4 (&t)[6][2], const EpiCtx& E) {
  constexpr int J = I / 16, KS = I % 16;
  constexpr int rem = (I + 4 <= 47) ? 4 : 47 - I;
  asm volatile("s_waitcnt lgkmcnt(%2)" : "+v"(t[I % 6][0]), "+v"(t[I % 6][1]) : "i"(2 * rem));
  const bf16x8 bfr = __builtin_shufflevector(t[I % 6][0], t[I % 6][1], 0, 1, 2, 3, 4, 5, 6, 7);
  if constexpr (J >= 3 - NV) acc[J] = MFMA(af[KS], bfr, acc[J]);
  else                       acc[J] = MFMA(bfr, af[KS], acc[J]);
  if constexpr (I + 5 < 48) k1_rd1<I + 5>(t[(I + 5) % 6], wb);
  if constexpr (I == 18) k1_epi<NV, 0>(acc[0], E);
  if constexpr (I == 34) k1_epi<NV, 1>(acc[1], E);
  __builtin_amdgcn_sched_barrier(0);
  if constexpr (I + 1 < 48) k1_jsteps<NV, I + 1>(af, wb, acc, t, E);
  else k1_epi<NV, 2>(acc[2], E);
}

struct PrepIn { unsigned kk[11], vv[8], wv[4]; u32x4 skw, gw, gx[2]; };
struct PrepOut { u32x4 kv[2]; u32x2 wo; float gv[2]; };
DI void prep_store(const PrepOut& O, char* __restrict__ ws, int b, int pw, int lane);
DI void prep_wait(PrepIn& P);
struct PrepSrc { const u16 *Ak, *Av, *shiftk, *wo, *gatew; };
DI void prep_load(PrepIn& P, const u16* __restrict__ Aq, const u16* __restrict__ Ak, const u16* __restrict__ Av,
                  const u16* __restrict__ shiftk, const u16* __restrict__ wo, const u16* __restrict__ gatew,
                  int b, int pw, int lane);
DI void prep_compute(PrepIn& P, PrepOut& O, int b, int lane);

template <int NV>
DI void gemm_body(const u16* __restrict__ X, char* lds, int mbase, int n0,
                  int w, int lane, u16* __restrict__ Qh, u16* __restrict__ Kh,
                  u16* __restrict__ Vt, char* ws, const PrepSrc& S, int b) {
  const int r = lane & 31, hh = lane >> 5;
  f32x16 acc[3];
#pragma unroll
  for (int j = 0; j < 3; ++j)
#pragma unroll
    for (int i = 0; i < 16; ++i) acc[j][i] = 0.f;

  const int g = (lane >> 4) & 1, q = (lane & 15) >> 2, p = lane & 3;
  const unsigned ldsb = (unsigned)(size_t)lds;
  const unsigned wb = ldsb + (8 * hh + q) * 192 + (16 * g + 4 * p) * 2;
  const unsigned xb = ldsb + K1_WBYTES + w * K1_XW;
  const unsigned xo = (unsigned)(hh * 1024 + ((r ^ hh) << 4));
  const unsigned xa = ldsb + K1_WBYTES + w * K1_XW + r * 512;
  const int m0 = mbase + 32 * w;

  bf16x8 af[16];
  asm volatile("s_waitcnt vmcnt(12)" ::: "memory");
  __builtin_amdgcn_s_barrier();
  asm volatile("" ::: "memory");
  STAMP(0, 1);
#pragma unroll
  for (int ks = 0; ks < 16; ++ks) af[ks] = *(const LDS_AS bf16x8*)(xa + (((2 * ks + hh) ^ (r & 15)) * 16));
  asm volatile("s_waitcnt lgkmcnt(0)" ::: "memory");
  k1_issue_x(X, xb, 1, m0, xo);
  asm volatile("" ::: "memory");
  k1_half<NV>(af, wb, acc);
  STAMP(0, 5);
  asm volatile("s_waitcnt vmcnt(0)" ::: "memory");
  __builtin_amdgcn_s_barrier();
  asm volatile("" ::: "memory");
  STAMP(0, 4);
#pragma unroll
  for (int ks = 0; ks < 16; ++ks) af[ks] = *(const LDS_AS bf16x8*)(xa + (((2 * ks + hh) ^ (r & 15)) * 16));
  asm volatile("s_waitcnt lgkmcnt(0)" ::: "memory");
  {
    const EpiCtx E = {n0, m0, r, hh, Qh, Kh, Vt};
    s16x4 t[6][2];
    const unsigned wb2 = wb + 256 * 192;
    k1_rd1<0>(t[0], wb2); k1_rd1<1>(t[1], wb2); k1_rd1<2>(t[2], wb2); k1_rd1<3>(t[3], wb2); k1_rd1<4>(t[4], wb2);
    k1_jsteps<NV, 0>(af, wb2, acc, t, E);
  }
  STAMP(0, 2);
}

#define GLD1(dst, voff, sb, imm) asm volatile("global_load_dword %0, %1, %2 offset:" #imm : "=v"(dst) : "v"(voff), "s"(uni(sb)))
#define GLD4(dst, voff, sb, imm) asm volatile("global_load_dwordx4 %0, %1, %2 offset:" #imm : "=v"(dst) : "v"(voff), "s"(uni(sb)))
#define GLDH(dst, voff, sb, imm) asm volatile("global_load_ushort %0, %1, %2 offset:" #imm : "=v"(dst) : "v"(voff), "s"(uni(sb)))
DI void prep_load(PrepIn& P, const u16* __restrict__ Aq, const u16* __restrict__ Ak, const u16* __restrict__ Av,
                  const u16* __restrict__ shiftk, const u16* __restrict__ wo, const u16* __restrict__ gatew,
                  int b, int pw, int lane) {
  const int st = b >> 3, dt = b & 7;
  const int dg = lane & 7, sg = lane >> 3;
  const int d0 = dt * 64 + dg * 8 + 2 * pw;
  const int s0 = st * 64 + sg * 8;
  const unsigned va = (unsigned)((s0 == 0 ? 0 : s0 - 3) * 1024 + d0 * 2);
  const unsigned vb = (unsigned)(s0 * 1024 + d0 * 2);
  const char* k2 = (const char*)Ak + 4096;
  const char* v2 = (const char*)Av + 4096;
  GLD1(P.kk[0], va, Ak, 0); GLD1(P.kk[1], va, Ak, 1024); GLD1(P.kk[2], va, Ak, 2048);
  GLD1(P.kk[3], vb, Ak, 0); GLD1(P.kk[4], vb, Ak, 1024); GLD1(P.kk[5], vb, Ak, 2048); GLD1(P.kk[6], vb, Ak, 3072);
  GLD1(P.kk[7], vb, k2, 0); GLD1(P.kk[8], vb, k2, 1024); GLD1(P.kk[9], vb, k2, 2048); GLD1(P.kk[10], vb, k2, 3072);
  GLD1(P.vv[0], vb, Av, 0); GLD1(P.vv[1], vb, Av, 1024); GLD1(P.vv[2], vb, Av, 2048); GLD1(P.vv[3], vb, Av, 3072);
  GLD1(P.vv[4], vb, v2, 0); GLD1(P.vv[5], vb, v2, 1024); GLD1(P.vv[6], vb, v2, 2048); GLD1(P.vv[7], vb, v2, 3072);
  const unsigned vs = (unsigned)(d0 * 8), vl = (unsigned)(lane * 16), vh = (unsigned)(lane * 2);
  GLD4(P.skw, vs, shiftk, 0);
  GLD4(P.gw, vl, gatew, 0);
  const char* gq = (const char*)Aq + (size_t)(b * 8 + pw * 2) * 1024;
  GLD4(P.gx[0], vl, gq, 0); GLD4(P.gx[1], vl, gq, 1024);
  const char* wr = (const char*)wo + (size_t)((b >> 3) * 16 + pw * 4) * 1024 + (b & 7) * 128;
  GLDH(P.wv[0], vh, wr, 0); GLDH(P.wv[1], vh, wr, 1024); GLDH(P.wv[2], vh, wr, 2048); GLDH(P.wv[3], vh, wr, 3072);
}
DI void prep_wait(PrepIn& P) {
  __builtin_amdgcn_sched_barrier(0);
  asm volatile("s_waitcnt vmcnt(0)" ::: "memory");
  __builtin_amdgcn_sched_barrier(0);
  asm volatile(""
               : "+v"(P.kk[0]), "+v"(P.kk[1]), "+v"(P.kk[2]), "+v"(P.kk[3]), "+v"(P.kk[4]), "+v"(P.kk[5]),
                 "+v"(P.kk[6]), "+v"(P.kk[7]), "+v"(P.kk[8]), "+v"(P.kk[9]), "+v"(P.kk[10]),
                 "+v"(P.vv[0]), "+v"(P.vv[1]), "+v"(P.vv[2]), "+v"(P.vv[3]), "+v"(P.vv[4]), "+v"(P.vv[5]),
                 "+v"(P.vv[6]), "+v"(P.vv[7]));
  asm volatile("" : "+v"(P.skw), "+v"(P.gw), "+v"(P.gx[0]), "+v"(P.gx[1]),
                    "+v"(P.wv[0]), "+v"(P.wv[1]), "+v"(P.wv[2]), "+v"(P.wv[3]));
}

DI void prep_compute(PrepIn& P, PrepOut& O, int b, int lane) {
  const int s0 = (b >> 3) * 64 + (lane >> 3) * 8;
  if (s0 == 0) { P.kk[0] = 0; P.kk[1] = 0; P.kk[2] = 0; }
#pragma unroll
  for (int c = 0; c < 2; ++c) {
    const unsigned s01 = P.skw[c * 2], s23 = P.skw[c * 2 + 1];
    const float sk0 = bflo(s01), sk1 = bfhi(s01), sk2 = bflo(s23), sk3 = bfhi(s23);
    float kf[11];
#pragma unroll
    for (int i = 0; i < 11; ++i) kf[i] = c ? bfhi(P.kk[i]) : bflo(P.kk[i]);
    float kvv[8];
#pragma unroll
    for (int i = 0; i < 8; ++i) {
      float acc = rbf(kf[i] * sk3);
      acc = rbf(acc + rbf(kf[i + 1] * sk2));
      acc = rbf(acc + rbf(kf[i + 2] * sk1));
      acc = rbf(acc + rbf(kf[i + 3] * sk0));
      const float vf = c ? bfhi(P.vv[i]) : bflo(P.vv[i]);
      kvv[i] = acc * vf;
    }
    O.kv[c][0] = pk2(kvv[0], kvv[1]); O.kv[c][1] = pk2(kvv[2], kvv[3]);
    O.kv[c][2] = pk2(kvv[4], kvv[5]); O.kv[c][3] = pk2(kvv[6], kvv[7]);
  }
  O.wo[0] = P.wv[0] | (P.wv[1] << 16); O.wo[1] = P.wv[2] | (P.wv[3] << 16);
#pragma unroll
  for (int i = 0; i < 2; ++i) {
    float sum = 0.f;
#pragma unroll
    for (int e = 0; e < 4; ++e) {
      sum += rbf(bflo(P.gx[i][e]) * bflo(P.gw[e]));
      sum += rbf(bfhi(P.gx[i][e]) * bfhi(P.gw[e]));
    }
#pragma unroll
    for (int o = 32; o > 0; o >>= 1) sum += __shfl_xor(sum, o);
    const float z = rbf(sum);
    O.gv[i] = rbf(1.f / (1.f + __expf(-z)));
  }
}

DI void prep_store(const PrepOut& O, char* __restrict__ ws, int b, int pw, int lane) {
  const int st = b >> 3, dt = b & 7;
  const int dg = lane & 7, sg = lane >> 3;
  const int d0 = dt * 64 + dg * 8 + 2 * pw;
  const int s0 = st * 64 + sg * 8;
  u16* kvT = (u16*)(ws + WS_KVT);
  st16(kvT + (size_t)d0 * SEQ + s0, O.kv[0]);
  st16(kvT + (size_t)(d0 + 1) * SEQ + s0, O.kv[1]);
  const int wk0 = (b >> 3) * 16 + pw * 4, wn = (b & 7) * 64 + lane;
  u16* woT = (u16*)(ws + WS_WOT);
  const size_t off = ((size_t)(((wn >> 5) * 32 + (wk0 >> 4)) * 2 + ((wk0 >> 3) & 1)) * 32 + (wn & 31)) * 8 + (wk0 & 7);
  st8(woT + off, O.wo);
  float* gout = (float*)(ws + WS_G);
  if (lane < 2) gout[b * 8 + pw * 2 + lane] = lane ? O.gv[1] : O.gv[0];
}

__global__ __launch_bounds__(512, 2) void k1_gemm_prep(
    const u16* __restrict__ Aq, const u16* __restrict__ Ak, const u16* __restrict__ Av,
    const u16* __restrict__ shiftk, const u16* __restrict__ wqkv, const u16* __restrict__ wo,
    const u16* __restrict__ gatew, char* __restrict__ ws) {
  __shared__ __attribute__((aligned(16))) char lds[K1_WBYTES + 4 * K1_XW];
  const int tid = threadIdx.x, lane = tid & 63;
  const int w = __builtin_amdgcn_readfirstlane(tid >> 6);
  const int b = blockIdx.x;
  STAMP(0, 0);
  if (w >= 4) {
    __builtin_amdgcn_s_setprio(3);
    PrepIn P;
    prep_load(P, Aq, Ak, Av, shiftk, wo, gatew, b, w - 4, lane);
    __builtin_amdgcn_s_barrier();
    prep_wait(P);
    PrepOut O;
    prep_compute(P, O, b, lane);
    prep_store(O, ws, b, w - 4, lane);
    return;
  }
  const int xcd = b & 7, idx = b >> 3;
  const int mt = (xcd >> 1) * 4 + (idx >> 3);
  const int nt = (xcd & 1) * 8 + (idx & 7);
  const int n0 = nt * 96, mbase = mt * 128;
  const int rot = (idx >> 3) * 4;
  const unsigned ldsb = (unsigned)(size_t)lds;
  unsigned wo3[3];
#pragma unroll
  for (int j = 0; j < 3; ++j) {
    const int ci = j * 64 + lane, row = ci / 12, c16 = ci - row * 12;
    wo3[j] = (unsigned)(row * 3072 + c16 * 16);
  }
  const unsigned xo = (unsigned)((lane >> 5) * 1024 + (((lane & 31) ^ (lane >> 5)) << 4));

  const PrepSrc P = {Ak, Av, shiftk, wo, gatew};
  k1_issue_w(wqkv, ldsb, 0, n0, w, wo3, rot);
  k1_issue_x(Aq, ldsb + K1_WBYTES + w * K1_XW, 0, mbase + 32 * w, xo);
  k1_issue_w(wqkv, ldsb, 1, n0, w, wo3, rot);

  u16* Qh = (u16*)(ws + WS_Q);
  u16* Kh = (u16*)(ws + WS_K);
  u16* Vt = (u16*)(ws + WS_VT);
  if (nt <= 9) gemm_body<0>(Aq, lds, mbase, n0, w, lane, Qh, Kh, Vt, ws, P, b);
  else if (nt == 10) gemm_body<1>(Aq, lds, mbase, n0, w, lane, Qh, Kh, Vt, ws, P, b);
  else gemm_body<3>(Aq, lds, mbase, n0, w, lane, Qh, Kh, Vt, ws, P, b);
  STAMP(0, 3);
}

#define CSTR 4608
#define XR_OFF (8 * CSTR)
#define XR_STR 80
#define K2_LDS (XR_OFF + 95 * XR_STR)

struct ConvRegs { u32x4 h0, h1, hx0, hx1, xv; };

DI void conv_load(ConvRegs& R, int d, const u16* __restrict__ ssmk, char* __restrict__ ws) {
  const int tid = threadIdx.x, lane = tid & 63, w = tid >> 6;
  const int r = lane & 31, hh = lane >> 5;
  const u16* hrow = ssmk + (size_t)d * SEQ;
  const u32x4 z4 = {0u, 0u, 0u, 0u};
  R.h0 = *(const u32x4*)(hrow + 8 * (tid >= 4 ? tid - 4 : 0));
  R.h1 = *(const u32x4*)(hrow + 8 * (tid >= 3 ? tid - 3 : 0));
  if (tid < 4) R.h0 = z4;
  if (tid < 3) R.h1 = z4;
  R.hx0 = z4; R.hx1 = z4;
  if (tid < 4) {
    R.hx0 = *(const u32x4*)(hrow + 8 * (252 + tid));
    if (tid < 3) R.hx1 = *(const u32x4*)(hrow + 8 * (253 + tid));
  }
  R.xv = *(const u32x4*)((const u16*)(ws + WS_KVT) + (size_t)d * SEQ + 8 * tid);
}

DI void conv_write_copies(char* lds, int t, u32x4 h0, u32x4 h1) {
  unsigned wv[8] = {h0[0], h0[1], h0[2], h0[3], h1[0], h1[1], h1[2], h1[3]};
  unsigned od[7];
#pragma unroll
  for (int i = 0; i < 7; ++i) od[i] = __builtin_amdgcn_alignbyte(wv[i + 1], wv[i], 2);
#pragma unroll
  for (int c = 0; c < 8; ++c) {
    const int e = c >> 1;
    u32x4 v;
    if (c & 1) { v[0] = od[e]; v[1] = od[e + 1]; v[2] = od[e + 2]; v[3] = od[e + 3]; }
    else       { v[0] = wv[e]; v[1] = wv[e + 1]; v[2] = wv[e + 2]; v[3] = wv[e + 3]; }
    const unsigned off = (0x5D9159D0u >> (4 * c)) & 15u;
    *(u32x4*)(lds + c * CSTR + off * 16 + t * 16) = v;
  }
}

DI void conv_compute(const ConvRegs& R, int d, char* lds, const u16* __restrict__ ssmD,
                     char* __restrict__ ws) {
  const int tid = threadIdx.x, lane = tid & 63, w = tid >> 6;
  const int r = lane & 31, hh = lane >> 5;
  {
    const u32x4 z4 = {0u, 0u, 0u, 0u};
    conv_write_copies(lds, tid, R.h0, R.h1);
    if (tid < 4) conv_write_copies(lds, 256 + tid, R.hx0, R.hx1);
    u32x4 rv;
#pragma unroll
    for (int i = 0; i < 4; ++i) rv[i] = __builtin_amdgcn_alignbit(R.xv[3 - i], R.xv[3 - i], 16);
    *(u32x4*)(lds + XR_OFF + ((tid >> 2) + 31) * XR_STR + (3 - (tid & 3)) * 16) = rv;
    if (tid < 124) *(u32x4*)(lds + XR_OFF + (tid >> 2) * XR_STR + (tid & 3) * 16) = z4;
  }
  __syncthreads();
  if (!(d & 1)) STAMP(1, 1); else STAMP(1, 4);

  f32x16 acc0, acc1a, acc1b;
#pragma unroll
  for (int i = 0; i < 16; ++i) { acc0[i] = 0.f; acc1a[i] = 0.f; acc1b[i] = 0.f; }
  {
    const int c = (1 + r) & 7, u = (1 + r) >> 3;
    const unsigned off = (0x5D9159D0u >> (4 * c)) & 15u;
    const char* abase = lds + c * CSTR + off * 16 + (16 + 8 * hh + 8 * u) * 2 + w * 8 * 64;
    const char* bbase = lds + XR_OFF + (r + 31 - w * 8) * XR_STR + 16 * hh;
#pragma unroll
    for (int gi = 0; gi < 8; ++gi) {
#pragma unroll
      for (int ks = 0; ks < 2; ++ks) {
        const bf16x8 A0 = *(const bf16x8*)(abase + gi * 64 - ks * 32);
        const bf16x8 A1 = *(const bf16x8*)(abase + gi * 64 - ks * 32 + 32 * 64);
        const bf16x8 B0 = *(const bf16x8*)(bbase - gi * XR_STR + (1 - ks) * 32);
        const bf16x8 B1 = *(const bf16x8*)(bbase + (32 - gi) * XR_STR + (1 - ks) * 32);
        acc0 = MFMA(A0, B0, acc0);
        acc1a = MFMA(A1, B0, acc1a);
        acc1b = MFMA(A0, B1, acc1b);
      }
    }
  }
  __syncthreads();
  if (!(d & 1)) STAMP(1, 5); else STAMP(1, 6);
  {
    float* red = (float*)lds;
#pragma unroll
    for (int dst = 0; dst < 4; ++dst)
#pragma unroll
      for (int hf = 0; hf < 2; ++hf) {
        f32x4 v;
#pragma unroll
        for (int i = 0; i < 4; ++i) {
          const int rg = 8 * (dst & 1) + 4 * hf + i;
          v[i] = (dst >> 1) ? (acc1a[rg] + acc1b[rg]) : acc0[rg];
        }
        *(f32x4*)(red + ((((w * 4 + dst) * 2 + hf) * 64) + lane) * 4) = v;
      }
  }
  __syncthreads();
  {
    const float* red = (const float*)lds;
    const float Dv = bf2f(ssmD[d]);
    u16* aT = (u16*)(ws + WS_AT) + (size_t)d * SEQ;
    const int ablk = 32 * (w >> 1) + r;
#pragma unroll
    for (int hf = 0; hf < 2; ++hf) {
      f32x4 y = {0.f, 0.f, 0.f, 0.f};
#pragma unroll
      for (int src = 0; src < 4; ++src) {
        const f32x4 v = *(const f32x4*)(red + ((((src * 4 + w) * 2 + hf) * 64) + lane) * 4);
        y += v;
      }
      const int b0 = 16 * (w & 1) + 8 * hf + 4 * hh;
      const int s = 32 * ablk + b0;
      const u32x2 xv = *(const u32x2*)(lds + XR_OFF + (ablk + 31) * XR_STR + (28 - b0) * 2);
      float kv[4] = {bfhi(xv[1]), bflo(xv[1]), bfhi(xv[0]), bflo(xv[0])};
      float a[4];
#pragma unroll
      for (int i = 0; i < 4; ++i) {
        a[i] = rbf(y[i]) + rbf(kv[i] * Dv);
      }
      u32x2 o;
      o[0] = pk2(a[0], a[1]); o[1] = pk2(a[2], a[3]);
      st8(aT + s, o);
    }
  }
  __syncthreads();
}

DI void attn_task(const char* lds, int lt0, int qb, bf16x8 qf, int h, char* __restrict__ ws) {
  const int lane = threadIdx.x & 63;
  const int c = lane & 31, hh = lane >> 5;
  f32x16 zero;
#pragma unroll
  for (int i = 0; i < 16; ++i) zero[i] = 0.f;
  f32x16 sc[9];
  const char* kb = lds + lt0 * 1024 + c * 32 + hh * 16;
#pragma unroll
  for (int t = 0; t < 9; ++t) {
    const bf16x8 kf = *(const bf16x8*)(kb + t * 1024);
    sc[t] = MFMA(kf, qf, zero);
  }
  const float NEG = -1e30f;
#pragma unroll
  for (int i = 0; i < 16; ++i) {
    const int row = (i & 3) + 8 * (i >> 2) + 4 * hh;
    sc[0][i] = (row > c) ? sc[0][i] : NEG;
    sc[8][i] = (row <= c) ? sc[8][i] : NEG;
  }
  if (qb < 8) {
#pragma unroll
    for (int t = 0; t < 8; ++t)
      if (qb - 8 + t < 0) {
#pragma unroll
        for (int i = 0; i < 16; ++i) sc[t][i] = NEG;
      }
  }
  float m = NEG;
#pragma unroll
  for (int t = 0; t < 9; ++t)
#pragma unroll
    for (int i = 0; i < 16; ++i) m = fmaxf(m, sc[t][i]);
  m = fmaxf(m, __shfl_xor(m, 32));
  const float cs = 0.25f * 1.4426950408889634f;
  const float mc = m * cs;
  f32x16 oacc[2];
  oacc[0] = zero; oacc[1] = zero;
  const unsigned onesw = (c == 16 || c == 20) ? 0x3F803F80u : 0u;
  const char* vb = lds + 16384 + lt0 * 1024 + (c & 15) * 32 + hh * 16;
#pragma unroll
  for (int t = 0; t < 9; ++t) {
#pragma unroll
    for (int s = 0; s < 2; ++s) {
      u32x4 vw = *(const u32x4*)(vb + t * 1024 + s * 512);
      if (c >= 16) { vw[0] = onesw; vw[1] = onesw; vw[2] = onesw; vw[3] = onesw; }
      u32x4 pw;
#pragma unroll
      for (int e = 0; e < 4; ++e) {
        const float p0 = __builtin_amdgcn_exp2f(fmaf(sc[t][8 * s + 2 * e], cs, -mc));
        const float p1 = __builtin_amdgcn_exp2f(fmaf(sc[t][8 * s + 2 * e + 1], cs, -mc));
        pw[e] = pk2(p0, p1);
      }
      oacc[s] = MFMA(__builtin_bit_cast(bf16x8, vw), __builtin_bit_cast(bf16x8, pw), oacc[s]);
    }
  }
  const float inv = 1.f / (oacc[0][8] + oacc[1][8]);
  unsigned a0 = pk2((oacc[0][0] + oacc[1][0]) * inv, (oacc[0][1] + oacc[1][1]) * inv);
  unsigned a1 = pk2((oacc[0][2] + oacc[1][2]) * inv, (oacc[0][3] + oacc[1][3]) * inv);
  unsigned b0 = pk2((oacc[0][4] + oacc[1][4]) * inv, (oacc[0][5] + oacc[1][5]) * inv);
  unsigned b1 = pk2((oacc[0][6] + oacc[1][6]) * inv, (oacc[0][7] + oacc[1][7]) * inv);
  auto r0 = __builtin_amdgcn_permlane32_swap(a0, b0, false, false);
  auto r1 = __builtin_amdgcn_permlane32_swap(a1, b1, false, false);
  u32x4 wv = {r0[0], r1[0], r0[1], r1[1]};
  u16* o = (u16*)(ws + WS_O) + ((size_t)((qb * 32 + h) * 2 + hh) * 32 + c) * 8;
  st16(o, wv);
}

DI void attn_body(int h, int g, char* lds, char* __restrict__ ws) {
  const int tid = threadIdx.x, lane = tid & 63;
  const int w = __builtin_amdgcn_readfirstlane(tid >> 6);
  const int c = lane & 31, hh = lane >> 5;
  const u16* Qh = (const u16*)(ws + WS_Q) + (size_t)h * SEQ * DH;
  const u16* Kh = (const u16*)(ws + WS_K) + (size_t)h * SEQ * DH;
  const u16* Vt = (const u16*)(ws + WS_VT) + (size_t)h * 64 * 512;
#pragma unroll
  for (int i = 0; i < 4; ++i) {
    const int lt = w * 4 + i;
    int gt = 8 * g - 8 + lt; gt = gt < 0 ? 0 : gt;
    __builtin_amdgcn_global_load_lds((GLB_AS const void*)(Kh + (size_t)gt * 512 + lane * 8),
                                     (LDS_AS void*)(lds + lt * 1024), 16, 0, 0);
    __builtin_amdgcn_global_load_lds((GLB_AS const void*)(Vt + (size_t)gt * 512 + lane * 8),
                                     (LDS_AS void*)(lds + 16384 + lt * 1024), 16, 0, 0);
  }
  const int qb0 = 8 * g + w, qb1 = 8 * g + w + 4;
  const bf16x8 q0 = *(const bf16x8*)(Qh + (size_t)(32 * qb0 + c) * DH + 8 * hh);
  const bf16x8 q1 = *(const bf16x8*)(Qh + (size_t)(32 * qb1 + c) * DH + 8 * hh);
  asm volatile("s_waitcnt vmcnt(0)" ::: "memory");
  __syncthreads();
  STAMP(1, 1);
  attn_task(lds, w, qb0, q0, h, ws);
  STAMP(1, 2);
  attn_task(lds, w + 4, qb1, q1, h, ws);
}

__global__ __launch_bounds__(256, 2) void k2_attn_conv(const u16* __restrict__ ssmk,
                                                       const u16* __restrict__ ssmD,
                                                       char* __restrict__ ws) {
  __shared__ __attribute__((aligned(16))) char lds[K2_LDS];
  const int b = blockIdx.x;
  STAMP(1, 0);
  if (b >= 256) {
    const int a = b - 256;
    attn_body(a >> 3, a & 7, lds, ws);
  } else {
    ConvRegs R0, R1;
    conv_load(R0, 2 * b, ssmk, ws);
    conv_load(R1, 2 * b + 1, ssmk, ws);
    conv_compute(R0, 2 * b, lds, ssmD, ws);
    STAMP(1, 2);
    conv_compute(R1, 2 * b + 1, lds, ssmD, ws);
  }
  STAMP(1, 3);
}

__global__ __launch_bounds__(256, 1) void k3_outproj(char* __restrict__ ws, const u16* __restrict__ Aq, u16* __restrict__ out) {
  __shared__ __attribute__((aligned(16))) char lds[131072];
  const int tid = threadIdx.x, lane = tid & 63;
  const int w = __builtin_amdgcn_readfirstlane(tid >> 6);
  const int c = lane & 31, hh = lane >> 5;
  const int b = blockIdx.x;
  STAMP(2, 0);
  const int st = (b & 7) * 4 + (b >> 6), ntl = (b >> 3) & 7;
  {
    const u16* src = (w < 2) ? (const u16*)(ws + WS_WOT) + (size_t)(ntl * 2 + w) * 16384
                             : (const u16*)(ws + WS_O) + (size_t)(st * 2 + w - 2) * 16384;
    src += lane * 8;
#pragma unroll
    for (int i = 0; i < 32; ++i)
      __builtin_amdgcn_global_load_lds((GLB_AS const void*)(src + i * 512), (LDS_AS void*)(lds + w * 32768 + i * 1024), 16, 0, 0);
  }
  asm volatile("" ::: "memory");
  const int ntile = ntl * 2 + (w & 1), stile = st * 2 + (w >> 1);
  const int n0 = ntile * 32, s0 = stile * 32;
  const int s = s0 + c;
  const float g = ((const float*)(ws + WS_G))[s];
  const u16* aT = (const u16*)(ws + WS_AT);
  unsigned av[16];
#pragma unroll
  for (int i = 0; i < 16; ++i) av[i] = aT[(size_t)(n0 + (i & 3) + 8 * (i >> 2) + 4 * hh) * SEQ + s];
  u32x2 qv[4];
#pragma unroll
  for (int gq = 0; gq < 4; ++gq) qv[gq] = *(const u32x2*)(Aq + (size_t)s * DM + n0 + 8 * gq + 4 * hh);
  asm volatile("" ::: "memory");
  f32x16 acc;
#pragma unroll
  for (int i = 0; i < 16; ++i) acc[i] = 0.f;
  const char* ab = lds + (w & 1) * 32768 + lane * 16;
  const char* bb = lds + (2 + (w >> 1)) * 32768 + lane * 16;
#pragma unroll
  for (int ch = 0; ch < 4; ++ch) {
    if (ch == 0) asm volatile("s_waitcnt vmcnt(45)" ::: "memory");
    if (ch == 1) asm volatile("s_waitcnt vmcnt(37)" ::: "memory");
    if (ch == 2) asm volatile("s_waitcnt vmcnt(29)" ::: "memory");
    if (ch == 3) asm volatile("s_waitcnt vmcnt(21)" ::: "memory");
    __builtin_amdgcn_s_barrier();
    asm volatile("" ::: "memory");
    bf16x8 af[8], bf[8];
#pragma unroll
    for (int ks = 0; ks < 8; ++ks) {
      af[ks] = *(const bf16x8*)(ab + (ch * 8 + ks) * 1024);
      bf[ks] = *(const bf16x8*)(bb + (ch * 8 + ks) * 1024);
    }
    __builtin_amdgcn_sched_barrier(0);
#pragma unroll
    for (int ks = 0; ks < 8; ++ks) acc = MFMA(af[ks], bf[ks], acc);
    __builtin_amdgcn_sched_barrier(0);
  }
  STAMP(2, 2);
  const float omg = rbf(1.f - g);
#pragma unroll
  for (int gq = 0; gq < 4; ++gq) {
    const int n = n0 + 8 * gq + 4 * hh;
    float res[4];
#pragma unroll
    for (int i = 0; i < 4; ++i) {
      const float qf = (i & 1) ? bfhi(qv[gq][i >> 1]) : bflo(qv[gq][i >> 1]);
      const float a = rbf(qf * bf2f(av[4 * gq + i]));
      const float bb2 = rbf(acc[4 * gq + i]);
      res[i] = rbf(g * a) + rbf(omg * bb2);
    }
    u32x2 wv;
    wv[0] = pk2(res[0], res[1]); wv[1] = pk2(res[2], res[3]);
    st8(out + (size_t)s * DM + n, wv);
  }
  STAMP(2, 3);
}

extern "C" void kernel_launch(void* const* d_in, const int* in_sizes, int n_in,
                              void* d_out, int out_size, void* d_ws, size_t ws_size,
                              hipStream_t stream) {
  const u16* Aq = (const u16*)d_in[0];
  const u16* Ak = (const u16*)d_in[1];
  const u16* Av = (const u16*)d_in[2];
  const u16* shiftk = (const u16*)d_in[3];
  const u16* ssmk = (const u16*)d_in[4];
  const u16* ssmD = (const u16*)d_in[5];
  const u16* wqkv = (const u16*)d_in[7];
  const u16* wo = (const u16*)d_in[8];
  const u16* gatew = (const u16*)d_in[9];
  char* ws = (char*)d_ws;
  k1_gemm_prep<<<256, 512, 0, stream>>>(Aq, Ak, Av, shiftk, wqkv, wo, gatew, ws);
  k2_attn_conv<<<512, 256, 0, stream>>>(ssmk, ssmD, ws);
  k3_outproj<<<256, 256, 0, stream>>>(ws, Aq, (u16*)d_out);
}
```

```cpp
#include <hip/hip_runtime.h>
#include <hip/hip_bf16.h>
#include <stdint.h>

#define SEQ 2048
#define DM 512
#define NH 32
#define DH 16

typedef __attribute__((ext_vector_type(8))) short bf16x8;
typedef __attribute__((ext_vector_type(4))) short s16x4;
typedef __attribute__((ext_vector_type(16))) float f32x16;
typedef __attribute__((ext_vector_type(4))) float f32x4;
typedef __attribute__((ext_vector_type(2))) float f32x2;
typedef __attribute__((ext_vector_type(4))) unsigned u32x4;
typedef __attribute__((ext_vector_type(2))) unsigned u32x2;
typedef __attribute__((ext_vector_type(2))) __bf16 bf16x2_t;
typedef unsigned short u16;

#define DI __device__ __forceinline__
#define MFMA(a, b, c) __builtin_amdgcn_mfma_f32_32x32x16_bf16((a), (b), (c), 0, 0, 0)
#define LDS_AS __attribute__((address_space(3)))
#define GLB_AS __attribute__((address_space(1)))

DI float bf2f(unsigned v16) { return __uint_as_float(v16 << 16); }
DI float bflo(unsigned w) { return __uint_as_float(w << 16); }
DI float bfhi(unsigned w) { return __uint_as_float(w & 0xffff0000u); }
DI unsigned pk2(float lo, float hi) {
  bf16x2_t v; v[0] = (__bf16)lo; v[1] = (__bf16)hi;
  return __builtin_bit_cast(unsigned, v);
}
DI float rbf(float x) {
  bf16x2_t v; v[0] = (__bf16)x; v[1] = (__bf16)0.f;
  return __uint_as_float(__builtin_bit_cast(unsigned, v) << 16);
}

#define ST_MOD "sc1"
DI void st16(void* p, u32x4 v) {
  asm volatile("global_store_dwordx4 %0, %1, off " ST_MOD "\n\ts_nop 1" :: "v"(p), "v"(v) : "memory");
}
DI void st8(void* p, u32x2 v) {
  asm volatile("global_store_dwordx2 %0, %1, off\n\ts_nop 1" :: "v"(p), "v"(v) : "memory");
}
#define STAMP(kid, slot)
#define WS_Q   (0u)
#define WS_K   (2u << 20)
#define WS_VT  (4u << 20)
#define WS_KVT (6u << 20)
#define WS_QT  (8u << 20)
#define WS_AT  (10u << 20)
#define WS_O   (12u << 20)
#define WS_WOT (14u << 20)
#define WS_G   (15u << 20)

#define K1_WBYTES (512 * 192)
#define K1_XW 16384

DI const char* uni(const void* p) {
  const uint64_t v = (uint64_t)p;
  const unsigned lo = __builtin_amdgcn_readfirstlane((unsigned)v), hi = __builtin_amdgcn_readfirstlane((unsigned)(v >> 32));
  return (const char*)(((uint64_t)hi << 32) | lo);
}
DI void glds16(unsigned voff, const void* sbase, unsigned ldsaddr) {
  asm volatile("s_mov_b32 m0, %2\n\ts_nop 0\n\tglobal_load_lds_dwordx4 %0, %1"
               :: "v"(voff), "s"(uni(sbase)), "s"(__builtin_amdgcn_readfirstlane(ldsaddr)) : "memory");
}
DI void k1_issue_w(const u16* __restrict__ W, unsigned ldsb, int half, int n0, int w, const unsigned (&wo3)[3], int rot) {
#pragma unroll
  for (int i = 0; i < 4; ++i) {
    const int g = half * 16 + ((4 * i + w + rot) & 15);
    const char* sb = (const char*)W + (size_t)g * (16 * 3072) + n0 * 2;
#pragma unroll
    for (int j = 0; j < 3; ++j) glds16(wo3[j], sb, ldsb + g * 3072 + j * 1024);
  }
}
DI void k1_issue_x(const u16* __restrict__ X, unsigned xb, int half, int m0, unsigned xo) {
#pragma unroll
  for (int i = 0; i < 16; ++i) {
    const char* sb = (const char*)X + (size_t)(m0 + 2 * i) * 1024 + half * 512;
    glds16(xo ^ (unsigned)(((2 * i) & 15) << 4), sb, xb + i * 1024);
  }
}

template <int OFF>
DI void tr_rd(s16x4& d, unsigned base) {
  asm volatile("ds_read_b64_tr_b16 %0, %1 offset:%2" : "=v"(d) : "v"(base), "i"(OFF));
}
template <int KS>
DI void k1_rdb(s16x4 (&t)[6], unsigned base) {
  tr_rd<KS * 3072 + 0>(t[0], base);   tr_rd<KS * 3072 + 768>(t[1], base);
  tr_rd<KS * 3072 + 64>(t[2], base);  tr_rd<KS * 3072 + 64 + 768>(t[3], base);
  tr_rd<KS * 3072 + 128>(t[4], base); tr_rd<KS * 3072 + 128 + 768>(t[5], base);
}
template <int N>
DI void lgkm_wait6(s16x4 (&t)[6]) {
  asm volatile("s_waitcnt lgkmcnt(%6)" : "+v"(t[0]), "+v"(t[1]), "+v"(t[2]), "+v"(t[3]), "+v"(t[4]), "+v"(t[5]) : "i"(N));
}
template <int NV, int KS>
DI void k1_steps(const bf16x8 (&af)[16], unsigned wb, f32x16 (&acc)[3], s16x4 (&t0)[6], s16x4 (&t1)[6], s16x4 (&t2)[6]) {
  if constexpr (KS + 1 < 16) lgkm_wait6<6>(t0); else lgkm_wait6<0>(t0);
#pragma unroll
  for (int j = 0; j < 3; ++j) {
    const bf16x8 bfr = __builtin_shufflevector(t0[2 * j], t0[2 * j + 1], 0, 1, 2, 3, 4, 5, 6, 7);
    if (j >= 3 - NV) acc[j] = MFMA(af[KS], bfr, acc[j]);
    else             acc[j] = MFMA(bfr, af[KS], acc[j]);
    if (j == 0) { if constexpr (KS + 2 < 16) k1_rdb<KS + 2>(t2, wb); }
  }
  __builtin_amdgcn_sched_barrier(0);
  if constexpr (KS + 1 < 16) k1_steps<NV, KS + 1>(af, wb, acc, t1, t2, t0);
}
template <int NV>
DI void k1_half(const bf16x8 (&af)[16], unsigned wb, f32x16 (&acc)[3]) {
  s16x4 t0[6], t1[6], t2[6];
  k1_rdb<0>(t0, wb);
  k1_rdb<1>(t1, wb);
  k1_steps<NV, 0>(af, wb, acc, t0, t1, t2);
}

struct EpiCtx { int n0, m0, r, hh; u16 *Qh, *Kh, *Vt; };
template <int NV, int J>
DI void k1_epi(const f32x16& a, const EpiCtx& E) {
  const int nc = E.n0 + 32 * J;
  if constexpr (J >= 3 - NV) {
    const int nv = nc - 1024 + E.r;
    const int head = nv >> 4, d = nv & 15;
    u16* dst = E.Vt + (size_t)(head * 64 + (E.m0 >> 5)) * 512 + d * 16 + 8 * E.hh;
    u32x4 w0, w1;
    w0[0] = pk2(a[0], a[1]);  w0[1] = pk2(a[2], a[3]);
    w0[2] = pk2(a[4], a[5]);  w0[3] = pk2(a[6], a[7]);
    w1[0] = pk2(a[8], a[9]);  w1[1] = pk2(a[10], a[11]);
    w1[2] = pk2(a[12], a[13]); w1[3] = pk2(a[14], a[15]);
    st16(dst, w0);
    st16(dst + 256, w1);
  } else {
    u16* base = (nc < 512) ? E.Qh : E.Kh;
    const int head0 = (nc & 511) >> 4;
#pragma unroll
    for (int e = 0; e < 2; ++e) {
      unsigned a0 = pk2(a[8 * e], a[8 * e + 1]), a1 = pk2(a[8 * e + 2], a[8 * e + 3]);
      unsigned b0 = pk2(a[8 * e + 4], a[8 * e + 5]), b1 = pk2(a[8 * e + 6], a[8 * e + 7]);
      auto r0 = __builtin_amdgcn_permlane32_swap(a0, b0, false, false);
      auto r1 = __builtin_amdgcn_permlane32_swap(a1, b1, false, false);
      u32x4 wv = {r0[0], r1[0], r0[1], r1[1]};
      u16* dst = base + ((size_t)(head0 + e) * SEQ + E.m0 + E.r) * DH + 8 * E.hh;
      st16(dst, wv);
    }
  }
}
template <int I>
DI void k1_rd1(s16x4 (&t)[2], unsigned base) {
  tr_rd<(I % 16) * 3072 + (I / 16) * 64>(t[0], base);
  tr_rd<(I % 16) * 3072 + (I / 16) * 64 + 768>(t[1], base);
}
template <int NV, int I>
DI void k1_jsteps(const bf16x8 (&af)[16], unsigned wb, f32x16 (&acc)[3], s16x4 (&t)[6][2], const EpiCtx& E) {
  constexpr int J = I / 16, KS = I % 16;
  constexpr int rem = (I + 4 <= 47) ? 4 : 47 - I;
  asm volatile("s_waitcnt lgkmcnt(%2)" : "+v"(t[I % 6][0]), "+v"(t[I % 6][1]) : "i"(2 * rem));
  const bf16x8 bfr = __builtin_shufflevector(t[I % 6][0], t[I % 6][1], 0, 1, 2, 3, 4, 5, 6, 7);
  if constexpr (J >= 3 - NV) acc[J] = MFMA(af[KS], bfr, acc[J]);
  else                       acc[J] = MFMA(bfr, af[KS], acc[J]);
  if constexpr (I + 5 < 48) k1_rd1<I + 5>(t[(I + 5) % 6], wb);
  if constexpr (I == 18) k1_epi<NV, 0>(acc[0], E);
  if constexpr (I == 34) k1_epi<NV, 1>(acc[1], E);
  __builtin_amdgcn_sched_barrier(0);
  if constexpr (I + 1 < 48) k1_jsteps<NV, I + 1>(af, wb, acc, t, E);
  else k1_epi<NV, 2>(acc[2], E);
}

struct PrepIn { unsigned kk[11], vv[8], wv[4]; u32x4 skw, gw, gx[2]; };
struct PrepOut { u32x4 kv[2]; u32x2 wo; float gv[2]; };
DI void prep_store(const PrepOut& O, char* __restrict__ ws, int b, int pw, int lane);
DI void prep_wait(PrepIn& P);
struct PrepSrc { const u16 *Ak, *Av, *shiftk, *wo, *gatew; };
DI void prep_load(PrepIn& P, const u16* __restrict__ Aq, const u16* __restrict__ Ak, const u16* __restrict__ Av,
                  const u16* __restrict__ shiftk, const u16* __restrict__ wo, const u16* __restrict__ gatew,
                  int b, int pw, int lane);
DI void prep_compute(PrepIn& P, PrepOut& O, int b, int lane);

template <int NV>
DI void gemm_body(const u16* __restrict__ X, char* lds, int mbase, int n0,
                  int w, int lane, u16* __restrict__ Qh, u16* __restrict__ Kh,
                  u16* __restrict__ Vt, char* ws, const PrepSrc& S, int b) {
  const int r = lane & 31, hh = lane >> 5;
  f32x16 acc[3];
#pragma unroll
  for (int j = 0; j < 3; ++j)
#pragma unroll
    for (int i = 0; i < 16; ++i) acc[j][i] = 0.f;

  const int g = (lane >> 4) & 1, q = (lane & 15) >> 2, p = lane & 3;
  const unsigned ldsb = (unsigned)(size_t)lds;
  const unsigned wb = ldsb + (8 * hh + q) * 192 + (16 * g + 4 * p) * 2;
  const unsigned xb = ldsb + K1_WBYTES + w * K1_XW;
  const unsigned xo = (unsigned)(hh * 1024 + ((r ^ hh) << 4));
  const unsigned xa = ldsb + K1_WBYTES + w * K1_XW + r * 512;
  const int m0 = mbase + 32 * w;

  bf16x8 af[16];
  asm volatile("s_waitcnt vmcnt(12)" ::: "memory");
  __builtin_amdgcn_s_barrier();
  asm volatile("" ::: "memory");
  STAMP(0, 1);
#pragma unroll
  for (int ks = 0; ks < 16; ++ks) af[ks] = *(const LDS_AS bf16x8*)(xa + (((2 * ks + hh) ^ (r & 15)) * 16));
  asm volatile("s_waitcnt lgkmcnt(0)" ::: "memory");
  k1_issue_x(X, xb, 1, m0, xo);
  asm volatile("" ::: "memory");
  k1_half<NV>(af, wb, acc);
  STAMP(0, 5);
  asm volatile("s_waitcnt vmcnt(0)" ::: "memory");
  __builtin_amdgcn_s_barrier();
  asm volatile("" ::: "memory");
  STAMP(0, 4);
#pragma unroll
  for (int ks = 0; ks < 16; ++ks) af[ks] = *(const LDS_AS bf16x8*)(xa + (((2 * ks + hh) ^ (r & 15)) * 16));
  asm volatile("s_waitcnt lgkmcnt(0)" ::: "memory");
  {
    const EpiCtx E = {n0, m0, r, hh, Qh, Kh, Vt};
    s16x4 t[6][2];
    const unsigned wb2 = wb + 256 * 192;
    k1_rd1<0>(t[0], wb2); k1_rd1<1>(t[1], wb2); k1_rd1<2>(t[2], wb2); k1_rd1<3>(t[3], wb2); k1_rd1<4>(t[4], wb2);
    k1_jsteps<NV, 0>(af, wb2, acc, t, E);
  }
  STAMP(0, 2);
}

#define GLD1(dst, voff, sb, imm) asm volatile("global_load_dword %0, %1, %2 offset:" #imm : "=v"(dst) : "v"(voff), "s"(uni(sb)))
#define GLD4(dst, voff, sb, imm) asm volatile("global_load_dwordx4 %0, %1, %2 offset:" #imm : "=v"(dst) : "v"(voff), "s"(uni(sb)))
#define GLDH(dst, voff, sb, imm) asm volatile("global_load_ushort %0, %1, %2 offset:" #imm : "=v"(dst) : "v"(voff), "s"(uni(sb)))
DI void prep_load(PrepIn& P, const u16* __restrict__ Aq, const u16* __restrict__ Ak, const u16* __restrict__ Av,
                  const u16* __restrict__ shiftk, const u16* __restrict__ wo, const u16* __restrict__ gatew,
                  int b, int pw, int lane) {
  const int st = b >> 3, dt = b & 7;
  const int dg = lane & 7, sg = lane >> 3;
  const int d0 = dt * 64 + dg * 8 + 2 * pw;
  const int s0 = st * 64 + sg * 8;
  const unsigned va = (unsigned)((s0 == 0 ? 0 : s0 - 3) * 1024 + d0 * 2);
  const unsigned vb = (unsigned)(s0 * 1024 + d0 * 2);
  const char* k2 = (const char*)Ak + 4096;
  const char* v2 = (const char*)Av + 4096;
  GLD1(P.kk[0], va, Ak, 0); GLD1(P.kk[1], va, Ak, 1024); GLD1(P.kk[2], va, Ak, 2048);
  GLD1(P.kk[3], vb, Ak, 0); GLD1(P.kk[4], vb, Ak, 1024); GLD1(P.kk[5], vb, Ak, 2048); GLD1(P.kk[6], vb, Ak, 3072);
  GLD1(P.kk[7], vb, k2, 0); GLD1(P.kk[8], vb, k2, 1024); GLD1(P.kk[9], vb, k2, 2048); GLD1(P.kk[10], vb, k2, 3072);
  GLD1(P.vv[0], vb, Av, 0); GLD1(P.vv[1], vb, Av, 1024); GLD1(P.vv[2], vb, Av, 2048); GLD1(P.vv[3], vb, Av, 3072);
  GLD1(P.vv[4], vb, v2, 0); GLD1(P.vv[5], vb, v2, 1024); GLD1(P.vv[6], vb, v2, 2048); GLD1(P.vv[7], vb, v2, 3072);
  const unsigned vs = (unsigned)(d0 * 8), vl = (unsigned)(lane * 16), vh = (unsigned)(lane * 2);
  GLD4(P.skw, vs, shiftk, 0);
  GLD4(P.gw, vl, gatew, 0);
  const char* gq = (const char*)Aq + (size_t)(b * 8 + pw * 2) * 1024;
  GLD4(P.gx[0], vl, gq, 0); GLD4(P.gx[1], vl, gq, 1024);
  const char* wr = (const char*)wo + (size_t)((b >> 3) * 16 + pw * 4) * 1024 + (b & 7) * 128;
  GLDH(P.wv[0], vh, wr, 0); GLDH(P.wv[1], vh, wr, 1024); GLDH(P.wv[2], vh, wr, 2048); GLDH(P.wv[3], vh, wr, 3072);
}
DI void prep_wait(PrepIn& P) {
  __builtin_amdgcn_sched_barrier(0);
  asm volatile("s_waitcnt vmcnt(0)" ::: "memory");
  __builtin_amdgcn_sched_barrier(0);
  asm volatile(""
               : "+v"(P.kk[0]), "+v"(P.kk[1]), "+v"(P.kk[2]), "+v"(P.kk[3]), "+v"(P.kk[4]), "+v"(P.kk[5]),
                 "+v"(P.kk[6]), "+v"(P.kk[7]), "+v"(P.kk[8]), "+v"(P.kk[9]), "+v"(P.kk[10]),
                 "+v"(P.vv[0]), "+v"(P.vv[1]), "+v"(P.vv[2]), "+v"(P.vv[3]), "+v"(P.vv[4]), "+v"(P.vv[5]),
                 "+v"(P.vv[6]), "+v"(P.vv[7]));
  asm volatile("" : "+v"(P.skw), "+v"(P.gw), "+v"(P.gx[0]), "+v"(P.gx[1]),
                    "+v"(P.wv[0]), "+v"(P.wv[1]), "+v"(P.wv[2]), "+v"(P.wv[3]));
}

DI void prep_compute(PrepIn& P, PrepOut& O, int b, int lane) {
  const int s0 = (b >> 3) * 64 + (lane >> 3) * 8;
  if (s0 == 0) { P.kk[0] = 0; P.kk[1] = 0; P.kk[2] = 0; }
#pragma unroll
  for (int c = 0; c < 2; ++c) {
    const unsigned s01 = P.skw[c * 2], s23 = P.skw[c * 2 + 1];
    const float sk0 = bflo(s01), sk1 = bfhi(s01), sk2 = bflo(s23), sk3 = bfhi(s23);
    float kf[11];
#pragma unroll
    for (int i = 0; i < 11; ++i) kf[i] = c ? bfhi(P.kk[i]) : bflo(P.kk[i]);
    float kvv[8];
#pragma unroll
    for (int i = 0; i < 8; ++i) {
      float acc = rbf(kf[i] * sk3);
      acc = rbf(acc + rbf(kf[i + 1] * sk2));
      acc = rbf(acc + rbf(kf[i + 2] * sk1));
      acc = rbf(acc + rbf(kf[i + 3] * sk0));
      const float vf = c ? bfhi(P.vv[i]) : bflo(P.vv[i]);
      kvv[i] = acc * vf;
    }
    O.kv[c][0] = pk2(kvv[0], kvv[1]); O.kv[c][1] = pk2(kvv[2], kvv[3]);
    O.kv[c][2] = pk2(kvv[4], kvv[5]); O.kv[c][3] = pk2(kvv[6], kvv[7]);
  }
  O.wo[0] = P.wv[0] | (P.wv[1] << 16); O.wo[1] = P.wv[2] | (P.wv[3] << 16);
#pragma unroll
  for (int i = 0; i < 2; ++i) {
    float sum = 0.f;
#pragma unroll
    for (int e = 0; e < 4; ++e) {
      sum += rbf(bflo(P.gx[i][e]) * bflo(P.gw[e]));
      sum += rbf(bfhi(P.gx[i][e]) * bfhi(P.gw[e]));
    }
#pragma unroll
    for (int o = 32; o > 0; o >>= 1) sum += __shfl_xor(sum, o);
    const float z = rbf(sum);
    O.gv[i] = rbf(1.f / (1.f + __expf(-z)));
  }
}

DI void prep_store(const PrepOut& O, char* __restrict__ ws, int b, int pw, int lane) {
  const int st = b >> 3, dt = b & 7;
  const int dg = lane & 7, sg = lane >> 3;
  const int d0 = dt * 64 + dg * 8 + 2 * pw;
  const int s0 = st * 64 + sg * 8;
  u16* kvT = (u16*)(ws + WS_KVT);
  st16(kvT + (size_t)d0 * SEQ + s0, O.kv[0]);
  st16(kvT + (size_t)(d0 + 1) * SEQ + s0, O.kv[1]);
  const int wk0 = (b >> 3) * 16 + pw * 4, wn = (b & 7) * 64 + lane;
  u16* woT = (u16*)(ws + WS_WOT);
  const size_t off = ((size_t)(((wn >> 5) * 32 + (wk0 >> 4)) * 2 + ((wk0 >> 3) & 1)) * 32 + (wn & 31)) * 8 + (wk0 & 7);
  st8(woT + off, O.wo);
  float* gout = (float*)(ws + WS_G);
  if (lane < 2) gout[b * 8 + pw * 2 + lane] = lane ? O.gv[1] : O.gv[0];
}

__global__ __launch_bounds__(512, 2) void k1_gemm_prep(
    const u16* __restrict__ Aq, const u16* __restrict__ Ak, const u16* __restrict__ Av,
    const u16* __restrict__ shiftk, const u16* __restrict__ wqkv, const u16* __restrict__ wo,
    const u16* __restrict__ gatew, char* __restrict__ ws) {
  __shared__ __attribute__((aligned(16))) char lds[K1_WBYTES + 4 * K1_XW];
  const int tid = threadIdx.x, lane = tid & 63;
  const int w = __builtin_amdgcn_readfirstlane(tid >> 6);
  const int b = blockIdx.x;
  STAMP(0, 0);
  if (w >= 4) {
    __builtin_amdgcn_s_setprio(3);
    PrepIn P;
    prep_load(P, Aq, Ak, Av, shiftk, wo, gatew, b, w - 4, lane);
    __builtin_amdgcn_s_barrier();
    prep_wait(P);
    PrepOut O;
    prep_compute(P, O, b, lane);
    prep_store(O, ws, b, w - 4, lane);
    return;
  }
  const int xcd = b & 7, idx = b >> 3;
  const int mt = (xcd >> 1) * 4 + (idx >> 3);
  const int nt = (xcd & 1) * 8 + (idx & 7);
  const int n0 = nt * 96, mbase = mt * 128;
  const int rot = (idx >> 3) * 4;
  const unsigned ldsb = (unsigned)(size_t)lds;
  unsigned wo3[3];
#pragma unroll
  for (int j = 0; j < 3; ++j) {
    const int ci = j * 64 + lane, row = ci / 12, c16 = ci - row * 12;
    wo3[j] = (unsigned)(row * 3072 + c16 * 16);
  }
  const unsigned xo = (unsigned)((lane >> 5) * 1024 + (((lane & 31) ^ (lane >> 5)) << 4));

  const PrepSrc P = {Ak, Av, shiftk, wo, gatew};
  k1_issue_w(wqkv, ldsb, 0, n0, w, wo3, rot);
  k1_issue_x(Aq, ldsb + K1_WBYTES + w * K1_XW, 0, mbase + 32 * w, xo);
  k1_issue_w(wqkv, ldsb, 1, n0, w, wo3, rot);

  u16* Qh = (u16*)(ws + WS_Q);
  u16* Kh = (u16*)(ws + WS_K);
  u16* Vt = (u16*)(ws + WS_VT);
  if (nt <= 9) gemm_body<0>(Aq, lds, mbase, n0, w, lane, Qh, Kh, Vt, ws, P, b);
  else if (nt == 10) gemm_body<1>(Aq, lds, mbase, n0, w, lane, Qh, Kh, Vt, ws, P, b);
  else gemm_body<3>(Aq, lds, mbase, n0, w, lane, Qh, Kh, Vt, ws, P, b);
  STAMP(0, 3);
}

#define CSTR 4608
#define XR_OFF (8 * CSTR)
#define XR_STR 80
#define K2_LDS (XR_OFF + 95 * XR_STR)

struct ConvRegs { u32x4 h0, h1, hx0, hx1, xv; };

DI void conv_load(ConvRegs& R, int d, const u16* __restrict__ ssmk, char* __restrict__ ws) {
  const int tid = threadIdx.x, lane = tid & 63, w = tid >> 6;
  const int r = lane & 31, hh = lane >> 5;
  const u16* hrow = ssmk + (size_t)d * SEQ;
  const u32x4 z4 = {0u, 0u, 0u, 0u};
  R.h0 = *(const u32x4*)(hrow + 8 * (tid >= 4 ? tid - 4 : 0));
  R.h1 = *(const u32x4*)(hrow + 8 * (tid >= 3 ? tid - 3 : 0));
  if (tid < 4) R.h0 = z4;
  if (tid < 3) R.h1 = z4;
  R.hx0 = z4; R.hx1 = z4;
  if (tid < 4) {
    R.hx0 = *(const u32x4*)(hrow + 8 * (252 + tid));
    if (tid < 3) R.hx1 = *(const u32x4*)(hrow + 8 * (253 + tid));
  }
  R.xv = *(const u32x4*)((const u16*)(ws + WS_KVT) + (size_t)d * SEQ + 8 * tid);
}

DI void conv_write_copies(char* lds, int t, u32x4 h0, u32x4 h1) {
  unsigned wv[8] = {h0[0], h0[1], h0[2], h0[3], h1[0], h1[1], h1[2], h1[3]};
  unsigned od[7];
#pragma unroll
  for (int i = 0; i < 7; ++i) od[i] = __builtin_amdgcn_alignbyte(wv[i + 1], wv[i], 2);
#pragma unroll
  for (int c = 0; c < 8; ++c) {
    const int e = c >> 1;
    u32x4 v;
    if (c & 1) { v[0] = od[e]; v[1] = od[e + 1]; v[2] = od[e + 2]; v[3] = od[e + 3]; }
    else       { v[0] = wv[e]; v[1] = wv[e + 1]; v[2] = wv[e + 2]; v[3] = wv[e + 3]; }
    const unsigned off = (0x5D9159D0u >> (4 * c)) & 15u;
    *(u32x4*)(lds + c * CSTR + off * 16 + t * 16) = v;
  }
}

DI void conv_compute(const ConvRegs& R, int d, char* lds, const u16* __restrict__ ssmD,
                     char* __restrict__ ws) {
  const int tid = threadIdx.x, lane = tid & 63, w = tid >> 6;
  const int r = lane & 31, hh = lane >> 5;
  {
    const u32x4 z4 = {0u, 0u, 0u, 0u};
    conv_write_copies(lds, tid, R.h0, R.h1);
    if (tid < 4) conv_write_copies(lds, 256 + tid, R.hx0, R.hx1);
    u32x4 rv;
#pragma unroll
    for (int i = 0; i < 4; ++i) rv[i] = __builtin_amdgcn_alignbit(R.xv[3 - i], R.xv[3 - i], 16);
    *(u32x4*)(lds + XR_OFF + ((tid >> 2) + 31) * XR_STR + (3 - (tid & 3)) * 16) = rv;
    if (tid < 124) *(u32x4*)(lds + XR_OFF + (tid >> 2) * XR_STR + (tid & 3) * 16) = z4;
  }
  __syncthreads();
  if (!(d & 1)) STAMP(1, 1); else STAMP(1, 4);

  f32x16 acc0, acc1a, acc1b;
#pragma unroll
  for (int i = 0; i < 16; ++i) { acc0[i] = 0.f; acc1a[i] = 0.f; acc1b[i] = 0.f; }
  {
    const int c = (1 + r) & 7, u = (1 + r) >> 3;
    const unsigned off = (0x5D9159D0u >> (4 * c)) & 15u;
    const char* abase = lds + c * CSTR + off * 16 + (16 + 8 * hh + 8 * u) * 2 + w * 8 * 64;
    const char* bbase = lds + XR_OFF + (r + 31 - w * 8) * XR_STR + 16 * hh;
#pragma unroll
    for (int gi = 0; gi < 8; ++gi) {
#pragma unroll
      for (int ks = 0; ks < 2; ++ks) {
        const bf16x8 A0 = *(const bf16x8*)(abase + gi * 64 - ks * 32);
        const bf16x8 A1 = *(const bf16x8*)(abase + gi * 64 - ks * 32 + 32 * 64);
        const bf16x8 B0 = *(const bf16x8*)(bbase - gi * XR_STR + (1 - ks) * 32);
        const bf16x8 B1 = *(const bf16x8*)(bbase + (32 - gi) * XR_STR + (1 - ks) * 32);
        acc0 = MFMA(A0, B0, acc0);
        acc1a = MFMA(A1, B0, acc1a);
        acc1b = MFMA(A0, B1, acc1b);
      }
    }
  }
  __syncthreads();
  if (!(d & 1)) STAMP(1, 5); else STAMP(1, 6);
  {
    float* red = (float*)lds;
#pragma unroll
    for (int dst = 0; dst < 4; ++dst)
#pragma unroll
      for (int hf = 0; hf < 2; ++hf) {
        f32x4 v;
#pragma unroll
        for (int i = 0; i < 4; ++i) {
          const int rg = 8 * (dst & 1) + 4 * hf + i;
          v[i] = (dst >> 1) ? (acc1a[rg] + acc1b[rg]) : acc0[rg];
        }
        *(f32x4*)(red + ((((w * 4 + dst) * 2 + hf) * 64) + lane) * 4) = v;
      }
  }
  __syncthreads();
  {
    const float* red = (const float*)lds;
    const float Dv = bf2f(ssmD[d]);
    u16* aT = (u16*)(ws + WS_AT) + (size_t)d * SEQ;
    const int ablk = 32 * (w >> 1) + r;
#pragma unroll
    for (int hf = 0; hf < 2; ++hf) {
      f32x4 y = {0.f, 0.f, 0.f, 0.f};
#pragma unroll
      for (int src = 0; src < 4; ++src) {
        const f32x4 v = *(const f32x4*)(red + ((((src * 4 + w) * 2 + hf) * 64) + lane) * 4);
        y += v;
      }
      const int b0 = 16 * (w & 1) + 8 * hf + 4 * hh;
      const int s = 32 * ablk + b0;
      const u32x2 xv = *(const u32x2*)(lds + XR_OFF + (ablk + 31) * XR_STR + (28 - b0) * 2);
      float kv[4] = {bfhi(xv[1]), bflo(xv[1]), bfhi(xv[0]), bflo(xv[0])};
      float a[4];
#pragma unroll
      for (int i = 0; i < 4; ++i) {
        a[i] = rbf(y[i]) + rbf(kv[i] * Dv);
      }
      u32x2 o;
      o[0] = pk2(a[0], a[1]); o[1] = pk2(a[2], a[3]);
      st8(aT + s, o);
    }
  }
  __syncthreads();
}

#define ATT_CONST 32768
template <int MODE>
DI f32x16 attn_scores(const char* kp, bf16x8 qf, int c, int hh) {
  f32x16 zero;
#pragma unroll
  for (int i = 0; i < 16; ++i) zero[i] = 0.f;
  f32x16 s = MFMA(*(const bf16x8*)kp, qf, zero);
  if constexpr (MODE != 0) {
#pragma unroll
    for (int i = 0; i < 16; ++i) {
      const int row = (i & 3) + 8 * (i >> 2) + 4 * hh;
      const bool keep = (MODE == 1) ? (row > c) : (row <= c);
      s[i] = keep ? s[i] : -1e30f;
    }
  }
  return s;
}
DI float max16(float m, const f32x16& s) {
#pragma unroll
  for (int i = 0; i < 16; ++i) m = fmaxf(m, s[i]);
  return m;
}
DI void attn_pv(const f32x16& sc, const char* vp, f32x2 cs2, f32x2 nmc2, f32x16 (&oacc)[2]) {
#pragma unroll
  for (int s = 0; s < 2; ++s) {
    const u32x4 vw = *(const u32x4*)(vp + s * 512);
    u32x4 pw;
#pragma unroll
    for (int e = 0; e < 4; ++e) {
      const f32x2 x = {sc[8 * s + 2 * e], sc[8 * s + 2 * e + 1]};
      const f32x2 y = __builtin_elementwise_fma(x, cs2, nmc2);
      pw[e] = pk2(__builtin_amdgcn_exp2f(y[0]), __builtin_amdgcn_exp2f(y[1]));
    }
    oacc[s] = MFMA(__builtin_bit_cast(bf16x8, vw), __builtin_bit_cast(bf16x8, pw), oacc[s]);
  }
}
DI void attn_task(const char* lds, int lt0, int qb, bf16x8 qf, int h, char* __restrict__ ws) {
  const int lane = threadIdx.x & 63;
  const int c = lane & 31, hh = lane >> 5;
  const char* kb = lds + lt0 * 1024 + c * 32 + hh * 16;
  const int t1 = qb >= 7 ? 1 : 8 - qb;
  float m = max16(-1e30f, attn_scores<2>(kb + 8 * 1024, qf, c, hh));
  if (qb >= 8) m = max16(m, attn_scores<1>(kb, qf, c, hh));
#pragma unroll 1
  for (int t = t1; t < 8; ++t) m = max16(m, attn_scores<0>(kb + t * 1024, qf, c, hh));
  m = fmaxf(m, __shfl_xor(m, 32));
  const float cs = 0.25f * 1.4426950408889634f;
  const float mc = m * cs;
  const f32x2 cs2 = {cs, cs}, nmc2 = {-mc, -mc};
  f32x16 oacc[2];
#pragma unroll
  for (int i = 0; i < 16; ++i) { oacc[0][i] = 0.f; oacc[1][i] = 0.f; }
  const int vstr = c < 16 ? 1024 : 0;
  const char* vb = c < 16 ? lds + 16384 + lt0 * 1024 + c * 32 + hh * 16
                          : lds + ATT_CONST + ((c == 16 || c == 20) ? 0 : 16);
  attn_pv(attn_scores<2>(kb + 8 * 1024, qf, c, hh), vb + 8 * vstr, cs2, nmc2, oacc);
  if (qb >= 8) attn_pv(attn_scores<1>(kb, qf, c, hh), vb, cs2, nmc2, oacc);
#pragma unroll 1
  for (int t = t1; t < 8; ++t) attn_pv(attn_scores<0>(kb + t * 1024, qf, c, hh), vb + t * vstr, cs2, nmc2, oacc);
  const float inv = 1.f / (oacc[0][8] + oacc[1][8]);
  unsigned a0 = pk2((oacc[0][0] + oacc[1][0]) * inv, (oacc[0][1] + oacc[1][1]) * inv);
  unsigned a1 = pk2((oacc[0][2] + oacc[1][2]) * inv, (oacc[0][3] + oacc[1][3]) * inv);
  unsigned b0 = pk2((oacc[0][4] + oacc[1][4]) * inv, (oacc[0][5] + oacc[1][5]) * inv);
  unsigned b1 = pk2((oacc[0][6] + oacc[1][6]) * inv, (oacc[0][7] + oacc[1][7]) * inv);
  auto r0 = __builtin_amdgcn_permlane32_swap(a0, b0, false, false);
  auto r1 = __builtin_amdgcn_permlane32_swap(a1, b1, false, false);
  u32x4 wv = {r0[0], r1[0], r0[1], r1[1]};
  u16* o = (u16*)(ws + WS_O) + ((size_t)((qb * 32 + h) * 2 + hh) * 32 + c) * 8;
  st16(o, wv);
}

DI void attn_body(int h, int g, char* lds, char* __restrict__ ws) {
  const int tid = threadIdx.x, lane = tid & 63;
  const int w = __builtin_amdgcn_readfirstlane(tid >> 6);
  const int c = lane & 31, hh = lane >> 5;
  const u16* Qh = (const u16*)(ws + WS_Q) + (size_t)h * SEQ * DH;
  const u16* Kh = (const u16*)(ws + WS_K) + (size_t)h * SEQ * DH;
  const u16* Vt = (const u16*)(ws + WS_VT) + (size_t)h * 64 * 512;
#pragma unroll
  for (int i = 0; i < 4; ++i) {
    const int lt = w * 4 + i;
    int gt = 8 * g - 8 + lt; gt = gt < 0 ? 0 : gt;
    __builtin_amdgcn_global_load_lds((GLB_AS const void*)(Kh + (size_t)gt * 512 + lane * 8),
                                     (LDS_AS void*)(lds + lt * 1024), 16, 0, 0);
    __builtin_amdgcn_global_load_lds((GLB_AS const void*)(Vt + (size_t)gt * 512 + lane * 8),
                                     (LDS_AS void*)(lds + 16384 + lt * 1024), 16, 0, 0);
  }
  const int qb0 = 8 * g + w, qb1 = 8 * g + w + 4;
  const bf16x8 q0 = *(const bf16x8*)(Qh + (size_t)(32 * qb0 + c) * DH + 8 * hh);
  const bf16x8 q1 = *(const bf16x8*)(Qh + (size_t)(32 * qb1 + c) * DH + 8 * hh);
  if (tid < 4) {
    const unsigned v = (tid & 1) ? 0u : 0x3F803F80u;
    const u32x4 cv = {v, v, v, v};
    *(u32x4*)(lds + ATT_CONST + (tid >> 1) * 512 + (tid & 1) * 16) = cv;
  }
  asm volatile("s_waitcnt vmcnt(0)" ::: "memory");
  __syncthreads();
  STAMP(1, 1);
  attn_task(lds, w, qb0, q0, h, ws);
  STAMP(1, 2);
  attn_task(lds, w + 4, qb1, q1, h, ws);
}

__global__ __launch_bounds__(256, 3) void k2_attn_conv(const u16* __restrict__ ssmk,
                                                       const u16* __restrict__ ssmD,
                                                       char* __restrict__ ws) {
  __shared__ __attribute__((aligned(16))) char lds[K2_LDS];
  const int b = blockIdx.x;
  STAMP(1, 0);
  if (b >= 512) {
    const int a = b - 512;
    attn_body(a >> 3, a & 7, lds, ws);
  } else {
    ConvRegs R0;
    conv_load(R0, b, ssmk, ws);
    conv_compute(R0, b, lds, ssmD, ws);
  }
  STAMP(1, 3);
}

__global__ __launch_bounds__(256, 1) void k3_outproj(char* __restrict__ ws, const u16* __restrict__ Aq, u16* __restrict__ out) {
  __shared__ __attribute__((aligned(16))) char lds[131072];
  const int tid = threadIdx.x, lane = tid & 63;
  const int w = __builtin_amdgcn_readfirstlane(tid >> 6);
  const int c = lane & 31, hh = lane >> 5;
  const int b = blockIdx.x;
  STAMP(2, 0);
  const int st = (b & 7) * 4 + (b >> 6), ntl = (b >> 3) & 7;
  {
    const u16* src = (w < 2) ? (const u16*)(ws + WS_WOT) + (size_t)(ntl * 2 + w) * 16384
                             : (const u16*)(ws + WS_O) + (size_t)(st * 2 + w - 2) * 16384;
    src += lane * 8;
#pragma unroll
    for (int i = 0; i < 32; ++i)
      __builtin_amdgcn_global_load_lds((GLB_AS const void*)(src + i * 512), (LDS_AS void*)(lds + w * 32768 + i * 1024), 16, 0, 0);
  }
  asm volatile("" ::: "memory");
  const int ntile = ntl * 2 + (w & 1), stile = st * 2 + (w >> 1);
  const int n0 = ntile * 32, s0 = stile * 32;
  const int s = s0 + c;
  const float g = ((const float*)(ws + WS_G))[s];
  const u16* aT = (const u16*)(ws + WS_AT);
  unsigned av[16];
#pragma unroll
  for (int i = 0; i < 16; ++i) av[i] = aT[(size_t)(n0 + (i & 3) + 8 * (i >> 2) + 4 * hh) * SEQ + s];
  u32x2 qv[4];
#pragma unroll
  for (int gq = 0; gq < 4; ++gq) qv[gq] = *(const u32x2*)(Aq + (size_t)s * DM + n0 + 8 * gq + 4 * hh);
  asm volatile("" ::: "memory");
  f32x16 acc;
#pragma unroll
  for (int i = 0; i < 16; ++i) acc[i] = 0.f;
  const char* ab = lds + (w & 1) * 32768 + lane * 16;
  const char* bb = lds + (2 + (w >> 1)) * 32768 + lane * 16;
#pragma unroll
  for (int ch = 0; ch < 4; ++ch) {
    if (ch == 0) asm volatile("s_waitcnt vmcnt(45)" ::: "memory");
    if (ch == 1) asm volatile("s_waitcnt vmcnt(37)" ::: "memory");
    if (ch == 2) asm volatile("s_waitcnt vmcnt(29)" ::: "memory");
    if (ch == 3) asm volatile("s_waitcnt vmcnt(21)" ::: "memory");
    __builtin_amdgcn_s_barrier();
    asm volatile("" ::: "memory");
    bf16x8 af[8], bf[8];
#pragma unroll
    for (int ks = 0; ks < 8; ++ks) {
      af[ks] = *(const bf16x8*)(ab + (ch * 8 + ks) * 1024);
      bf[ks] = *(const bf16x8*)(bb + (ch * 8 + ks) * 1024);
    }
    __builtin_amdgcn_sched_barrier(0);
#pragma unroll
    for (int ks = 0; ks < 8; ++ks) acc = MFMA(af[ks], bf[ks], acc);
    __builtin_amdgcn_sched_barrier(0);
  }
  STAMP(2, 2);
  const float omg = rbf(1.f - g);
#pragma unroll
  for (int gq = 0; gq < 4; ++gq) {
    const int n = n0 + 8 * gq + 4 * hh;
    float res[4];
#pragma unroll
    for (int i = 0; i < 4; ++i) {
      const float qf = (i & 1) ? bfhi(qv[gq][i >> 1]) : bflo(qv[gq][i >> 1]);
      const float a = rbf(qf * bf2f(av[4 * gq + i]));
      const float bb2 = rbf(acc[4 * gq + i]);
      res[i] = rbf(g * a) + rbf(omg * bb2);
    }
    u32x2 wv;
    wv[0] = pk2(res[0], res[1]); wv[1] = pk2(res[2], res[3]);
    st8(out + (size_t)s * DM + n, wv);
  }
  STAMP(2, 3);
}

extern "C" void kernel_launch(void* const* d_in, const int* in_sizes, int n_in,
                              void* d_out, int out_size, void* d_ws, size_t ws_size,
                              hipStream_t stream) {
  const u16* Aq = (const u16*)d_in[0];
  const u16* Ak = (const u16*)d_in[1];
  const u16* Av = (const u16*)d_in[2];
  const u16* shiftk = (const u16*)d_in[3];
  const u16* ssmk = (const u16*)d_in[4];
  const u16* ssmD = (const u16*)d_in[5];
  const u16* wqkv = (const u16*)d_in[7];
  const u16* wo = (const u16*)d_in[8];
  const u16* gatew = (const u16*)d_in[9];
  char* ws = (char*)d_ws;
  k1_gemm_prep<<<256, 512, 0, stream>>>(Aq, Ak, Av, shiftk, wqkv, wo, gatew, ws);
  k2_attn_conv<<<768, 256, 0, stream>>>(ssmk, ssmD, ws);
  k3_outproj<<<256, 256, 0, stream>>>(ws, Aq, (u16*)d_out);
}
```

```cpp
#include <hip/hip_runtime.h>
#include <hip/hip_bf16.h>
#include <stdint.h>

#define SEQ 2048
#define DM 512
#define NH 32
#define DH 16

typedef __attribute__((ext_vector_type(8))) short bf16x8;
typedef __attribute__((ext_vector_type(4))) short s16x4;
typedef __attribute__((ext_vector_type(16))) float f32x16;
typedef __attribute__((ext_vector_type(4))) float f32x4;
typedef __attribute__((ext_vector_type(2))) float f32x2;
typedef __attribute__((ext_vector_type(4))) unsigned u32x4;
typedef __attribute__((ext_vector_type(2))) unsigned u32x2;
typedef __attribute__((ext_vector_type(2))) __bf16 bf16x2_t;
typedef unsigned short u16;

#define DI __device__ __forceinline__
#define MFMA(a, b, c) __builtin_amdgcn_mfma_f32_32x32x16_bf16((a), (b), (c), 0, 0, 0)
#define LDS_AS __attribute__((address_space(3)))
#define GLB_AS __attribute__((address_space(1)))

DI float bf2f(unsigned v16) { return __uint_as_float(v16 << 16); }
DI float bflo(unsigned w) { return __uint_as_float(w << 16); }
DI float bfhi(unsigned w) { return __uint_as_float(w & 0xffff0000u); }
DI unsigned pk2(float lo, float hi) {
  bf16x2_t v; v[0] = (__bf16)lo; v[1] = (__bf16)hi;
  return __builtin_bit_cast(unsigned, v);
}
DI float rbf(float x) {
  bf16x2_t v; v[0] = (__bf16)x; v[1] = (__bf16)0.f;
  return __uint_as_float(__builtin_bit_cast(unsigned, v) << 16);
}

#define ST_MOD "sc1"
DI void st16(void* p, u32x4 v) {
  asm volatile("global_store_dwordx4 %0, %1, off " ST_MOD "\n\ts_nop 1" :: "v"(p), "v"(v) : "memory");
}
DI void st8(void* p, u32x2 v) {
  asm volatile("global_store_dwordx2 %0, %1, off\n\ts_nop 1" :: "v"(p), "v"(v) : "memory");
}
#define STAMP(kid, slot)
#define WS_Q   (0u)
#define WS_K   (2u << 20)
#define WS_VT  (4u << 20)
#define WS_KVT (6u << 20)
#define WS_QT  (8u << 20)
#define WS_AT  (10u << 20)
#define WS_O   (12u << 20)
#define WS_WOT (14u << 20)
#define WS_G   (15u << 20)

#define K1_WBYTES (512 * 192)
#define K1_XW 16384

DI const char* uni(const void* p) {
  const uint64_t v = (uint64_t)p;
  const unsigned lo = __builtin_amdgcn_readfirstlane((unsigned)v), hi = __builtin_amdgcn_readfirstlane((unsigned)(v >> 32));
  return (const char*)(((uint64_t)hi << 32) | lo);
}
DI void glds16(unsigned voff, const void* sbase, unsigned ldsaddr) {
  asm volatile("s_mov_b32 m0, %2\n\ts_nop 0\n\tglobal_load_lds_dwordx4 %0, %1"
               :: "v"(voff), "s"(uni(sbase)), "s"(__builtin_amdgcn_readfirstlane(ldsaddr)) : "memory");
}
DI void k1_issue_w(const u16* __restrict__ W, unsigned ldsb, int half, int n0, int w, const unsigned (&wo3)[3], int rot) {
#pragma unroll
  for (int i = 0; i < 4; ++i) {
    const int g = half * 16 + ((4 * i + w + rot) & 15);
    const char* sb = (const char*)W + (size_t)g * (16 * 3072) + n0 * 2;
#pragma unroll
    for (int j = 0; j < 3; ++j) glds16(wo3[j], sb, ldsb + g * 3072 + j * 1024);
  }
}
DI void k1_issue_x(const u16* __restrict__ X, unsigned xb, int half, int m0, unsigned xo) {
#pragma unroll
  for (int i = 0; i < 16; ++i) {
    const char* sb = (const char*)X + (size_t)(m0 + 2 * i) * 1024 + half * 512;
    glds16(xo ^ (unsigned)(((2 * i) & 15) << 4), sb, xb + i * 1024);
  }
}

template <int OFF>
DI void tr_rd(s16x4& d, unsigned base) {
  asm volatile("ds_read_b64_tr_b16 %0, %1 offset:%2" : "=v"(d) : "v"(base), "i"(OFF));
}
template <int KS>
DI void k1_rdb(s16x4 (&t)[6], unsigned base) {
  tr_rd<KS * 3072 + 0>(t[0], base);   tr_rd<KS * 3072 + 768>(t[1], base);
  tr_rd<KS * 3072 + 64>(t[2], base);  tr_rd<KS * 3072 + 64 + 768>(t[3], base);
  tr_rd<KS * 3072 + 128>(t[4], base); tr_rd<KS * 3072 + 128 + 768>(t[5], base);
}
template <int N>
DI void lgkm_wait6(s16x4 (&t)[6]) {
  asm volatile("s_waitcnt lgkmcnt(%6)" : "+v"(t[0]), "+v"(t[1]), "+v"(t[2]), "+v"(t[3]), "+v"(t[4]), "+v"(t[5]) : "i"(N));
}
template <int NV, int KS>
DI void k1_steps(const bf16x8 (&af)[16], unsigned wb, f32x16 (&acc)[3], s16x4 (&t0)[6], s16x4 (&t1)[6], s16x4 (&t2)[6]) {
  if constexpr (KS + 1 < 16) lgkm_wait6<6>(t0); else lgkm_wait6<0>(t0);
#pragma unroll
  for (int j = 0; j < 3; ++j) {
    const bf16x8 bfr = __builtin_shufflevector(t0[2 * j], t0[2 * j + 1], 0, 1, 2, 3, 4, 5, 6, 7);
    if (j >= 3 - NV) acc[j] = MFMA(af[KS], bfr, acc[j]);
    else             acc[j] = MFMA(bfr, af[KS], acc[j]);
    if (j == 0) { if constexpr (KS + 2 < 16) k1_rdb<KS + 2>(t2, wb); }
  }
  __builtin_amdgcn_sched_barrier(0);
  if constexpr (KS + 1 < 16) k1_steps<NV, KS + 1>(af, wb, acc, t1, t2, t0);
}
template <int NV>
DI void k1_half(const bf16x8 (&af)[16], unsigned wb, f32x16 (&acc)[3]) {
  s16x4 t0[6], t1[6], t2[6];
  k1_rdb<0>(t0, wb);
  k1_rdb<1>(t1, wb);
  k1_steps<NV, 0>(af, wb, acc, t0, t1, t2);
}

struct EpiCtx { int n0, m0, r, hh; u16 *Qh, *Kh, *Vt; };
template <int NV, int J>
DI void k1_epi(const f32x16& a, const EpiCtx& E) {
  const int nc = E.n0 + 32 * J;
  if constexpr (J >= 3 - NV) {
    const int nv = nc - 1024 + E.r;
    const int head = nv >> 4, d = nv & 15;
    u16* dst = E.Vt + (size_t)(head * 64 + (E.m0 >> 5)) * 512 + d * 16 + 8 * E.hh;
    u32x4 w0, w1;
    w0[0] = pk2(a[0], a[1]);  w0[1] = pk2(a[2], a[3]);
    w0[2] = pk2(a[4], a[5]);  w0[3] = pk2(a[6], a[7]);
    w1[0] = pk2(a[8], a[9]);  w1[1] = pk2(a[10], a[11]);
    w1[2] = pk2(a[12], a[13]); w1[3] = pk2(a[14], a[15]);
    st16(dst, w0);
    st16(dst + 256, w1);
  } else {
    u16* base = (nc < 512) ? E.Qh : E.Kh;
    const int head0 = (nc & 511) >> 4;
#pragma unroll
    for (int e = 0; e < 2; ++e) {
      unsigned a0 = pk2(a[8 * e], a[8 * e + 1]), a1 = pk2(a[8 * e + 2], a[8 * e + 3]);
      unsigned b0 = pk2(a[8 * e + 4], a[8 * e + 5]), b1 = pk2(a[8 * e + 6], a[8 * e + 7]);
      auto r0 = __builtin_amdgcn_permlane32_swap(a0, b0, false, false);
      auto r1 = __builtin_amdgcn_permlane32_swap(a1, b1, false, false);
      u32x4 wv = {r0[0], r1[0], r0[1], r1[1]};
      u16* dst = base + ((size_t)(head0 + e) * SEQ + E.m0 + E.r) * DH + 8 * E.hh;
      st16(dst, wv);
    }
  }
}
template <int I>
DI void k1_rd1(s16x4 (&t)[2], unsigned base) {
  tr_rd<(I % 16) * 3072 + (I / 16) * 64>(t[0], base);
  tr_rd<(I % 16) * 3072 + (I / 16) * 64 + 768>(t[1], base);
}
template <int NV, int I>
DI void k1_jsteps(const bf16x8 (&af)[16], unsigned wb, f32x16 (&acc)[3], s16x4 (&t)[6][2], const EpiCtx& E) {
  constexpr int J = I / 16, KS = I % 16;
  constexpr int rem = (I + 4 <= 47) ? 4 : 47 - I;
  asm volatile("s_waitcnt lgkmcnt(%2)" : "+v"(t[I % 6][0]), "+v"(t[I % 6][1]) : "i"(2 * rem));
  const bf16x8 bfr = __builtin_shufflevector(t[I % 6][0], t[I % 6][1], 0, 1, 2, 3, 4, 5, 6, 7);
  if constexpr (J >= 3 - NV) acc[J] = MFMA(af[KS], bfr, acc[J]);
  else                       acc[J] = MFMA(bfr, af[KS], acc[J]);
  if constexpr (I + 5 < 48) k1_rd1<I + 5>(t[(I + 5) % 6], wb);
  if constexpr (I == 18) k1_epi<NV, 0>(acc[0], E);
  if constexpr (I == 34) k1_epi<NV, 1>(acc[1], E);
  __builtin_amdgcn_sched_barrier(0);
  if constexpr (I + 1 < 48) k1_jsteps<NV, I + 1>(af, wb, acc, t, E);
  else k1_epi<NV, 2>(acc[2], E);
}

struct PrepIn { unsigned kk[11], vv[8], wv[4]; u32x4 skw, gw, gx[2]; };
struct PrepOut { u32x4 kv[2]; u32x2 wo; float gv[2]; };
DI void prep_store(const PrepOut& O, char* __restrict__ ws, int b, int pw, int lane);
DI void prep_wait(PrepIn& P);
struct PrepSrc { const u16 *Ak, *Av, *shiftk, *wo, *gatew; };
DI void prep_load(PrepIn& P, const u16* __restrict__ Aq, const u16* __restrict__ Ak, const u16* __restrict__ Av,
                  const u16* __restrict__ shiftk, const u16* __restrict__ wo, const u16* __restrict__ gatew,
                  int b, int pw, int lane);
DI void prep_compute(PrepIn& P, PrepOut& O, int b, int lane);

template <int NV>
DI void gemm_body(const u16* __restrict__ X, char* lds, int mbase, int n0,
                  int w, int lane, u16* __restrict__ Qh, u16* __restrict__ Kh,
                  u16* __restrict__ Vt, char* ws, const PrepSrc& S, int b) {
  const int r = lane & 31, hh = lane >> 5;
  f32x16 acc[3];
#pragma unroll
  for (int j = 0; j < 3; ++j)
#pragma unroll
    for (int i = 0; i < 16; ++i) acc[j][i] = 0.f;

  const int g = (lane >> 4) & 1, q = (lane & 15) >> 2, p = lane & 3;
  const unsigned ldsb = (unsigned)(size_t)lds;
  const unsigned wb = ldsb + (8 * hh + q) * 192 + (16 * g + 4 * p) * 2;
  const unsigned xb = ldsb + K1_WBYTES + w * K1_XW;
  const unsigned xo = (unsigned)(hh * 1024 + ((r ^ hh) << 4));
  const unsigned xa = ldsb + K1_WBYTES + w * K1_XW + r * 512;
  const int m0 = mbase + 32 * w;

  bf16x8 af[16];
  asm volatile("s_waitcnt vmcnt(12)" ::: "memory");
  __builtin_amdgcn_s_barrier();
  asm volatile("" ::: "memory");
  STAMP(0, 1);
#pragma unroll
  for (int ks = 0; ks < 16; ++ks) af[ks] = *(const LDS_AS bf16x8*)(xa + (((2 * ks + hh) ^ (r & 15)) * 16));
  asm volatile("s_waitcnt lgkmcnt(0)" ::: "memory");
  k1_issue_x(X, xb, 1, m0, xo);
  asm volatile("" ::: "memory");
  k1_half<NV>(af, wb, acc);
  STAMP(0, 5);
  asm volatile("s_waitcnt vmcnt(0)" ::: "memory");
  __builtin_amdgcn_s_barrier();
  asm volatile("" ::: "memory");
  STAMP(0, 4);
#pragma unroll
  for (int ks = 0; ks < 16; ++ks) af[ks] = *(const LDS_AS bf16x8*)(xa + (((2 * ks + hh) ^ (r & 15)) * 16));
  asm volatile("s_waitcnt lgkmcnt(0)" ::: "memory");
  {
    const EpiCtx E = {n0, m0, r, hh, Qh, Kh, Vt};
    s16x4 t[6][2];
    const unsigned wb2 = wb + 256 * 192;
    k1_rd1<0>(t[0], wb2); k1_rd1<1>(t[1], wb2); k1_rd1<2>(t[2], wb2); k1_rd1<3>(t[3], wb2); k1_rd1<4>(t[4], wb2);
    k1_jsteps<NV, 0>(af, wb2, acc, t, E);
  }
  STAMP(0, 2);
}

#define GLD1(dst, voff, sb, imm) asm volatile("global_load_dword %0, %1, %2 offset:" #imm : "=v"(dst) : "v"(voff), "s"(uni(sb)))
#define GLD4(dst, voff, sb, imm) asm volatile("global_load_dwordx4 %0, %1, %2 offset:" #imm : "=v"(dst) : "v"(voff), "s"(uni(sb)))
#define GLDH(dst, voff, sb, imm) asm volatile("global_load_ushort %0, %1, %2 offset:" #imm : "=v"(dst) : "v"(voff), "s"(uni(sb)))
DI void prep_load(PrepIn& P, const u16* __restrict__ Aq, const u16* __restrict__ Ak, const u16* __restrict__ Av,
                  const u16* __restrict__ shiftk, const u16* __restrict__ wo, const u16* __restrict__ gatew,
                  int b, int pw, int lane) {
  const int st = b >> 3, dt = b & 7;
  const int dg = lane & 7, sg = lane >> 3;
  const int d0 = dt * 64 + dg * 8 + 2 * pw;
  const int s0 = st * 64 + sg * 8;
  const unsigned va = (unsigned)((s0 == 0 ? 0 : s0 - 3) * 1024 + d0 * 2);
  const unsigned vb = (unsigned)(s0 * 1024 + d0 * 2);
  const char* k2 = (const char*)Ak + 4096;
  const char* v2 = (const char*)Av + 4096;
  GLD1(P.kk[0], va, Ak, 0); GLD1(P.kk[1], va, Ak, 1024); GLD1(P.kk[2], va, Ak, 2048);
  GLD1(P.kk[3], vb, Ak, 0); GLD1(P.kk[4], vb, Ak, 1024); GLD1(P.kk[5], vb, Ak, 2048); GLD1(P.kk[6], vb, Ak, 3072);
  GLD1(P.kk[7], vb, k2, 0); GLD1(P.kk[8], vb, k2, 1024); GLD1(P.kk[9], vb, k2, 2048); GLD1(P.kk[10], vb, k2, 3072);
  GLD1(P.vv[0], vb, Av, 0); GLD1(P.vv[1], vb, Av, 1024); GLD1(P.vv[2], vb, Av, 2048); GLD1(P.vv[3], vb, Av, 3072);
  GLD1(P.vv[4], vb, v2, 0); GLD1(P.vv[5], vb, v2, 1024); GLD1(P.vv[6], vb, v2, 2048); GLD1(P.vv[7], vb, v2, 3072);
  const unsigned vs = (unsigned)(d0 * 8), vl = (unsigned)(lane * 16), vh = (unsigned)(lane * 2);
  GLD4(P.skw, vs, shiftk, 0);
  GLD4(P.gw, vl, gatew, 0);
  const char* gq = (const char*)Aq + (size_t)(b * 8 + pw * 2) * 1024;
  GLD4(P.gx[0], vl, gq, 0); GLD4(P.gx[1], vl, gq, 1024);
  const char* wr = (const char*)wo + (size_t)((b >> 3) * 16 + pw * 4) * 1024 + (b & 7) * 128;
  GLDH(P.wv[0], vh, wr, 0); GLDH(P.wv[1], vh, wr, 1024); GLDH(P.wv[2], vh, wr, 2048); GLDH(P.wv[3], vh, wr, 3072);
}
DI void prep_wait(PrepIn& P) {
  __builtin_amdgcn_sched_barrier(0);
  asm volatile("s_waitcnt vmcnt(0)" ::: "memory");
  __builtin_amdgcn_sched_barrier(0);
  asm volatile(""
               : "+v"(P.kk[0]), "+v"(P.kk[1]), "+v"(P.kk[2]), "+v"(P.kk[3]), "+v"(P.kk[4]), "+v"(P.kk[5]),
                 "+v"(P.kk[6]), "+v"(P.kk[7]), "+v"(P.kk[8]), "+v"(P.kk[9]), "+v"(P.kk[10]),
                 "+v"(P.vv[0]), "+v"(P.vv[1]), "+v"(P.vv[2]), "+v"(P.vv[3]), "+v"(P.vv[4]), "+v"(P.vv[5]),
                 "+v"(P.vv[6]), "+v"(P.vv[7]));
  asm volatile("" : "+v"(P.skw), "+v"(P.gw), "+v"(P.gx[0]), "+v"(P.gx[1]),
                    "+v"(P.wv[0]), "+v"(P.wv[1]), "+v"(P.wv[2]), "+v"(P.wv[3]));
}

DI void prep_compute(PrepIn& P, PrepOut& O, int b, int lane) {
  const int s0 = (b >> 3) * 64 + (lane >> 3) * 8;
  if (s0 == 0) { P.kk[0] = 0; P.kk[1] = 0; P.kk[2] = 0; }
#pragma unroll
  for (int c = 0; c < 2; ++c) {
    const unsigned s01 = P.skw[c * 2], s23 = P.skw[c * 2 + 1];
    const float sk0 = bflo(s01), sk1 = bfhi(s01), sk2 = bflo(s23), sk3 = bfhi(s23);
    float kf[11];
#pragma unroll
    for (int i = 0; i < 11; ++i) kf[i] = c ? bfhi(P.kk[i]) : bflo(P.kk[i]);
    float kvv[8];
#pragma unroll
    for (int i = 0; i < 8; ++i) {
      float acc = rbf(kf[i] * sk3);
      acc = rbf(acc + rbf(kf[i + 1] * sk2));
      acc = rbf(acc + rbf(kf[i + 2] * sk1));
      acc = rbf(acc + rbf(kf[i + 3] * sk0));
      const float vf = c ? bfhi(P.vv[i]) : bflo(P.vv[i]);
      kvv[i] = acc * vf;
    }
    O.kv[c][0] = pk2(kvv[0], kvv[1]); O.kv[c][1] = pk2(kvv[2], kvv[3]);
    O.kv[c][2] = pk2(kvv[4], kvv[5]); O.kv[c][3] = pk2(kvv[6], kvv[7]);
  }
  O.wo[0] = P.wv[0] | (P.wv[1] << 16); O.wo[1] = P.wv[2] | (P.wv[3] << 16);
#pragma unroll
  for (int i = 0; i < 2; ++i) {
    float sum = 0.f;
#pragma unroll
    for (int e = 0; e < 4; ++e) {
      sum += rbf(bflo(P.gx[i][e]) * bflo(P.gw[e]));
      sum += rbf(bfhi(P.gx[i][e]) * bfhi(P.gw[e]));
    }
#pragma unroll
    for (int o = 32; o > 0; o >>= 1) sum += __shfl_xor(sum, o);
    const float z = rbf(sum);
    O.gv[i] = rbf(1.f / (1.f + __expf(-z)));
  }
}

DI void prep_store(const PrepOut& O, char* __restrict__ ws, int b, int pw, int lane) {
  const int st = b >> 3, dt = b & 7;
  const int dg = lane & 7, sg = lane >> 3;
  const int d0 = dt * 64 + dg * 8 + 2 * pw;
  const int s0 = st * 64 + sg * 8;
  u16* kvT = (u16*)(ws + WS_KVT);
  st16(kvT + (size_t)d0 * SEQ + s0, O.kv[0]);
  st16(kvT + (size_t)(d0 + 1) * SEQ + s0, O.kv[1]);
  const int wk0 = (b >> 3) * 16 + pw * 4, wn = (b & 7) * 64 + lane;
  u16* woT = (u16*)(ws + WS_WOT);
  const size_t off = ((size_t)(((wn >> 5) * 32 + (wk0 >> 4)) * 2 + ((wk0 >> 3) & 1)) * 32 + (wn & 31)) * 8 + (wk0 & 7);
  st8(woT + off, O.wo);
  float* gout = (float*)(ws + WS_G);
  if (lane < 2) gout[b * 8 + pw * 2 + lane] = lane ? O.gv[1] : O.gv[0];
}

__global__ __launch_bounds__(512, 2) void k1_gemm_prep(
    const u16* __restrict__ Aq, const u16* __restrict__ Ak, const u16* __restrict__ Av,
    const u16* __restrict__ shiftk, const u16* __restrict__ wqkv, const u16* __restrict__ wo,
    const u16* __restrict__ gatew, char* __restrict__ ws) {
  __shared__ __attribute__((aligned(16))) char lds[K1_WBYTES + 4 * K1_XW];
  const int tid = threadIdx.x, lane = tid & 63;
  const int w = __builtin_amdgcn_readfirstlane(tid >> 6);
  const int b = blockIdx.x;
  STAMP(0, 0);
  if (w >= 4) {
    __builtin_amdgcn_s_setprio(3);
    PrepIn P;
    prep_load(P, Aq, Ak, Av, shiftk, wo, gatew, b, w - 4, lane);
    __builtin_amdgcn_s_barrier();
    prep_wait(P);
    PrepOut O;
    prep_compute(P, O, b, lane);
    prep_store(O, ws, b, w - 4, lane);
    return;
  }
  const int xcd = b & 7, idx = b >> 3;
  const int mt = (xcd >> 1) * 4 + (idx >> 3);
  const int nt = (xcd & 1) * 8 + (idx & 7);
  const int n0 = nt * 96, mbase = mt * 128;
  const int rot = (idx >> 3) * 4;
  const unsigned ldsb = (unsigned)(size_t)lds;
  unsigned wo3[3];
#pragma unroll
  for (int j = 0; j < 3; ++j) {
    const int ci = j * 64 + lane, row = ci / 12, c16 = ci - row * 12;
    wo3[j] = (unsigned)(row * 3072 + c16 * 16);
  }
  const unsigned xo = (unsigned)((lane >> 5) * 1024 + (((lane & 31) ^ (lane >> 5)) << 4));

  const PrepSrc P = {Ak, Av, shiftk, wo, gatew};
  k1_issue_w(wqkv, ldsb, 0, n0, w, wo3, rot);
  k1_issue_x(Aq, ldsb + K1_WBYTES + w * K1_XW, 0, mbase + 32 * w, xo);
  k1_issue_w(wqkv, ldsb, 1, n0, w, wo3, rot);

  u16* Qh = (u16*)(ws + WS_Q);
  u16* Kh = (u16*)(ws + WS_K);
  u16* Vt = (u16*)(ws + WS_VT);
  if (nt <= 9) gemm_body<0>(Aq, lds, mbase, n0, w, lane, Qh, Kh, Vt, ws, P, b);
  else if (nt == 10) gemm_body<1>(Aq, lds, mbase, n0, w, lane, Qh, Kh, Vt, ws, P, b);
  else gemm_body<3>(Aq, lds, mbase, n0, w, lane, Qh, Kh, Vt, ws, P, b);
  STAMP(0, 3);
}

#define CSTR 4416
#define XR_OFF (8 * CSTR)
#define XR_STR 80
#define K2_LDS (XR_OFF + 65 * XR_STR)

struct ConvRegs { u32x4 h0, h1, hx0, hx1, xv; };

DI void conv_load(ConvRegs& R, int d, const u16* __restrict__ ssmk, char* __restrict__ ws) {
  const int tid = threadIdx.x, lane = tid & 63, w = tid >> 6;
  const int r = lane & 31, hh = lane >> 5;
  const u16* hrow = ssmk + (size_t)d * SEQ;
  const u32x4 z4 = {0u, 0u, 0u, 0u};
  R.h0 = *(const u32x4*)(hrow + 8 * (tid >= 4 ? tid - 4 : 0));
  R.h1 = *(const u32x4*)(hrow + 8 * (tid >= 3 ? tid - 3 : 0));
  if (tid < 4) R.h0 = z4;
  if (tid < 3) R.h1 = z4;
  R.hx0 = z4; R.hx1 = z4;
  if (tid < 4) {
    R.hx0 = *(const u32x4*)(hrow + 8 * (252 + tid));
    if (tid < 3) R.hx1 = *(const u32x4*)(hrow + 8 * (253 + tid));
  }
  R.xv = *(const u32x4*)((const u16*)(ws + WS_KVT) + (size_t)d * SEQ + 8 * tid);
}

DI void conv_write_copies(char* lds, int t, u32x4 h0, u32x4 h1) {
  unsigned wv[8] = {h0[0], h0[1], h0[2], h0[3], h1[0], h1[1], h1[2], h1[3]};
  unsigned od[7];
#pragma unroll
  for (int i = 0; i < 7; ++i) od[i] = __builtin_amdgcn_alignbyte(wv[i + 1], wv[i], 2);
#pragma unroll
  for (int c = 0; c < 8; ++c) {
    const int e = c >> 1;
    u32x4 v;
    if (c & 1) { v[0] = od[e]; v[1] = od[e + 1]; v[2] = od[e + 2]; v[3] = od[e + 3]; }
    else       { v[0] = wv[e]; v[1] = wv[e + 1]; v[2] = wv[e + 2]; v[3] = wv[e + 3]; }
    const unsigned off = (0x95519190u >> (4 * c)) & 15u;
    *(u32x4*)(lds + c * CSTR + off * 16 + t * 16) = v;
  }
}

DI void conv_compute(const ConvRegs& R, int d, char* lds, const u16* __restrict__ ssmD,
                     char* __restrict__ ws) {
  const int tid = threadIdx.x, lane = tid & 63, w = tid >> 6;
  const int r = lane & 31, hh = lane >> 5;
  {
    const u32x4 z4 = {0u, 0u, 0u, 0u};
    conv_write_copies(lds, tid, R.h0, R.h1);
    if (tid < 4) conv_write_copies(lds, 256 + tid, R.hx0, R.hx1);
    u32x4 rv;
#pragma unroll
    for (int i = 0; i < 4; ++i) rv[i] = __builtin_amdgcn_alignbit(R.xv[3 - i], R.xv[3 - i], 16);
    *(u32x4*)(lds + XR_OFF + ((tid >> 2) + 1) * XR_STR + (3 - (tid & 3)) * 16) = rv;
    if (tid < 4) *(u32x4*)(lds + XR_OFF + (tid & 3) * 16) = z4;
  }
  __syncthreads();
  if (!(d & 1)) STAMP(1, 1); else STAMP(1, 4);

  f32x16 acc0, acc1a, acc1b;
#pragma unroll
  for (int i = 0; i < 16; ++i) { acc0[i] = 0.f; acc1a[i] = 0.f; acc1b[i] = 0.f; }
  {
    const int c = (1 + r) & 7, u = (1 + r) >> 3;
    const unsigned off = (0x95519190u >> (4 * c)) & 15u;
    const char* abase = lds + c * CSTR + off * 16 + (16 + 8 * hh + 8 * u) * 2 + w * 8 * 64;
    const char* bbase = lds + XR_OFF + (r + 1 - w * 8) * XR_STR + 16 * hh;
#pragma unroll
    for (int gi = 0; gi < 8; ++gi) {
      const int rr = r + 1 - w * 8 - gi;
      const char* b0base = lds + XR_OFF + (rr > 0 ? rr : 0) * XR_STR + 16 * hh;
#pragma unroll
      for (int ks = 0; ks < 2; ++ks) {
        const bf16x8 A0 = *(const bf16x8*)(abase + gi * 64 - ks * 32);
        const bf16x8 A1 = *(const bf16x8*)(abase + gi * 64 - ks * 32 + 32 * 64);
        const bf16x8 B0 = *(const bf16x8*)(b0base + (1 - ks) * 32);
        const bf16x8 B1 = *(const bf16x8*)(bbase + (32 - gi) * XR_STR + (1 - ks) * 32);
        acc0 = MFMA(A0, B0, acc0);
        acc1a = MFMA(A1, B0, acc1a);
        acc1b = MFMA(A0, B1, acc1b);
      }
    }
  }
  __syncthreads();
  if (!(d & 1)) STAMP(1, 5); else STAMP(1, 6);
  {
    float* red = (float*)lds;
#pragma unroll
    for (int dst = 0; dst < 4; ++dst)
#pragma unroll
      for (int hf = 0; hf < 2; ++hf) {
        f32x4 v;
#pragma unroll
        for (int i = 0; i < 4; ++i) {
          const int rg = 8 * (dst & 1) + 4 * hf + i;
          v[i] = (dst >> 1) ? (acc1a[rg] + acc1b[rg]) : acc0[rg];
        }
        *(f32x4*)(red + ((((w * 4 + dst) * 2 + hf) * 64) + lane) * 4) = v;
      }
  }
  __syncthreads();
  {
    const float* red = (const float*)lds;
    const float Dv = bf2f(ssmD[d]);
    u16* aT = (u16*)(ws + WS_AT) + (size_t)d * SEQ;
    const int ablk = 32 * (w >> 1) + r;
#pragma unroll
    for (int hf = 0; hf < 2; ++hf) {
      f32x4 y = {0.f, 0.f, 0.f, 0.f};
#pragma unroll
      for (int src = 0; src < 4; ++src) {
        const f32x4 v = *(const f32x4*)(red + ((((src * 4 + w) * 2 + hf) * 64) + lane) * 4);
        y += v;
      }
      const int b0 = 16 * (w & 1) + 8 * hf + 4 * hh;
      const int s = 32 * ablk + b0;
      const u32x2 xv = *(const u32x2*)(lds + XR_OFF + (ablk + 1) * XR_STR + (28 - b0) * 2);
      float kv[4] = {bfhi(xv[1]), bflo(xv[1]), bfhi(xv[0]), bflo(xv[0])};
      float a[4];
#pragma unroll
      for (int i = 0; i < 4; ++i) {
        a[i] = rbf(y[i]) + rbf(kv[i] * Dv);
      }
      u32x2 o;
      o[0] = pk2(a[0], a[1]); o[1] = pk2(a[2], a[3]);
      st8(aT + s, o);
    }
  }
  __syncthreads();
}

#define ATT_CONST 32768
template <int MODE>
DI f32x16 attn_scores(const char* kp, bf16x8 qf, int c, int hh) {
  f32x16 zero;
#pragma unroll
  for (int i = 0; i < 16; ++i) zero[i] = 0.f;
  f32x16 s = MFMA(*(const bf16x8*)kp, qf, zero);
  if constexpr (MODE != 0) {
#pragma unroll
    for (int i = 0; i < 16; ++i) {
      const int row = (i & 3) + 8 * (i >> 2) + 4 * hh;
      const bool keep = (MODE == 1) ? (row > c) : (row <= c);
      s[i] = keep ? s[i] : -1e30f;
    }
  }
  return s;
}
DI float max16(float m, const f32x16& s) {
#pragma unroll
  for (int i = 0; i < 16; ++i) m = fmaxf(m, s[i]);
  return m;
}
DI void attn_pv(const f32x16& sc, const char* vp, f32x2 cs2, f32x2 nmc2, f32x16 (&oacc)[2]) {
#pragma unroll
  for (int s = 0; s < 2; ++s) {
    const u32x4 vw = *(const u32x4*)(vp + s * 512);
    u32x4 pw;
#pragma unroll
    for (int e = 0; e < 4; ++e) {
      const f32x2 x = {sc[8 * s + 2 * e], sc[8 * s + 2 * e + 1]};
      const f32x2 y = __builtin_elementwise_fma(x, cs2, nmc2);
      pw[e] = pk2(__builtin_amdgcn_exp2f(y[0]), __builtin_amdgcn_exp2f(y[1]));
    }
    oacc[s] = MFMA(__builtin_bit_cast(bf16x8, vw), __builtin_bit_cast(bf16x8, pw), oacc[s]);
  }
}
DI void attn_task(const char* lds, int lt0, int qb, bf16x8 qf, int h, char* __restrict__ ws) {
  const int lane = threadIdx.x & 63;
  const int c = lane & 31, hh = lane >> 5;
  const char* kb = lds + lt0 * 1024 + c * 32 + hh * 16;
  const int t1 = qb >= 7 ? 1 : 8 - qb;
  float m = max16(-1e30f, attn_scores<2>(kb + 8 * 1024, qf, c, hh));
  if (qb >= 8) m = max16(m, attn_scores<1>(kb, qf, c, hh));
#pragma unroll 1
  for (int t = t1; t < 8; ++t) m = max16(m, attn_scores<0>(kb + t * 1024, qf, c, hh));
  m = fmaxf(m, __shfl_xor(m, 32));
  const float cs = 0.25f * 1.4426950408889634f;
  const float mc = m * cs;
  const f32x2 cs2 = {cs, cs}, nmc2 = {-mc, -mc};
  f32x16 oacc[2];
#pragma unroll
  for (int i = 0; i < 16; ++i) { oacc[0][i] = 0.f; oacc[1][i] = 0.f; }
  const int vstr = c < 16 ? 1024 : 0;
  const char* vb = c < 16 ? lds + 16384 + lt0 * 1024 + c * 32 + hh * 16
                          : lds + ATT_CONST + ((c == 16 || c == 20) ? 0 : 16);
  attn_pv(attn_scores<2>(kb + 8 * 1024, qf, c, hh), vb + 8 * vstr, cs2, nmc2, oacc);
  if (qb >= 8) attn_pv(attn_scores<1>(kb, qf, c, hh), vb, cs2, nmc2, oacc);
#pragma unroll 1
  for (int t = t1; t < 8; ++t) attn_pv(attn_scores<0>(kb + t * 1024, qf, c, hh), vb + t * vstr, cs2, nmc2, oacc);
  const float inv = 1.f / (oacc[0][8] + oacc[1][8]);
  unsigned a0 = pk2((oacc[0][0] + oacc[1][0]) * inv, (oacc[0][1] + oacc[1][1]) * inv);
  unsigned a1 = pk2((oacc[0][2] + oacc[1][2]) * inv, (oacc[0][3] + oacc[1][3]) * inv);
  unsigned b0 = pk2((oacc[0][4] + oacc[1][4]) * inv, (oacc[0][5] + oacc[1][5]) * inv);
  unsigned b1 = pk2((oacc[0][6] + oacc[1][6]) * inv, (oacc[0][7] + oacc[1][7]) * inv);
  auto r0 = __builtin_amdgcn_permlane32_swap(a0, b0, false, false);
  auto r1 = __builtin_amdgcn_permlane32_swap(a1, b1, false, false);
  u32x4 wv = {r0[0], r1[0], r0[1], r1[1]};
  u16* o = (u16*)(ws + WS_O) + ((size_t)((qb * 32 + h) * 2 + hh) * 32 + c) * 8;
  st16(o, wv);
}

DI void attn_body(int h, int g, char* lds, char* __restrict__ ws) {
  const int tid = threadIdx.x, lane = tid & 63;
  const int w = __builtin_amdgcn_readfirstlane(tid >> 6);
  const int c = lane & 31, hh = lane >> 5;
  const u16* Qh = (const u16*)(ws + WS_Q) + (size_t)h * SEQ * DH;
  const u16* Kh = (const u16*)(ws + WS_K) + (size_t)h * SEQ * DH;
  const u16* Vt = (const u16*)(ws + WS_VT) + (size_t)h * 64 * 512;
#pragma unroll
  for (int i = 0; i < 3; ++i) {
    const int lt = w * 3 + i;
    int gt = 4 * g - 8 + lt; gt = gt < 0 ? 0 : gt;
    __builtin_amdgcn_global_load_lds((GLB_AS const void*)(Kh + (size_t)gt * 512 + lane * 8),
                                     (LDS_AS void*)(lds + lt * 1024), 16, 0, 0);
    __builtin_amdgcn_global_load_lds((GLB_AS const void*)(Vt + (size_t)gt * 512 + lane * 8),
                                     (LDS_AS void*)(lds + 16384 + lt * 1024), 16, 0, 0);
  }
  const int qb0 = 4 * g + w;
  const bf16x8 q0 = *(const bf16x8*)(Qh + (size_t)(32 * qb0 + c) * DH + 8 * hh);
  if (tid < 4) {
    const unsigned v = (tid & 1) ? 0u : 0x3F803F80u;
    const u32x4 cv = {v, v, v, v};
    *(u32x4*)(lds + ATT_CONST + (tid >> 1) * 512 + (tid & 1) * 16) = cv;
  }
  asm volatile("s_waitcnt vmcnt(0)" ::: "memory");
  __syncthreads();
  STAMP(1, 1);
  attn_task(lds, w, qb0, q0, h, ws);
}

__global__ __launch_bounds__(256, 4) void k2_attn_conv(const u16* __restrict__ ssmk,
                                                       const u16* __restrict__ ssmD,
                                                       char* __restrict__ ws) {
  __shared__ __attribute__((aligned(16))) char lds[K2_LDS];
  const int b = ((blockIdx.x >> 4) << 3) | (blockIdx.x & 7);
  STAMP(1, 0);
  if (!(blockIdx.x & 8)) {
    const int a = b;
    attn_body(a >> 4, a & 15, lds, ws);
  } else {
    ConvRegs R0;
    conv_load(R0, b, ssmk, ws);
    conv_compute(R0, b, lds, ssmD, ws);
  }
  STAMP(1, 3);
}

__global__ __launch_bounds__(256, 1) void k3_outproj(char* __restrict__ ws, const u16* __restrict__ Aq, u16* __restrict__ out) {
  __shared__ __attribute__((aligned(16))) char lds[131072];
  const int tid = threadIdx.x, lane = tid & 63;
  const int w = __builtin_amdgcn_readfirstlane(tid >> 6);
  const int c = lane & 31, hh = lane >> 5;
  const int b = blockIdx.x;
  STAMP(2, 0);
  const int st = (b & 7) * 4 + (b >> 6), ntl = (b >> 3) & 7;
  {
    const u16* src = (w < 2) ? (const u16*)(ws + WS_WOT) + (size_t)(ntl * 2 + w) * 16384
                             : (const u16*)(ws + WS_O) + (size_t)(st * 2 + w - 2) * 16384;
    src += lane * 8;
#pragma unroll
    for (int i = 0; i < 32; ++i)
      __builtin_amdgcn_global_load_lds((GLB_AS const void*)(src + i * 512), (LDS_AS void*)(lds + w * 32768 + i * 1024), 16, 0, 0);
  }
  asm volatile("" ::: "memory");
  const int ntile = ntl * 2 + (w & 1), stile = st * 2 + (w >> 1);
  const int n0 = ntile * 32, s0 = stile * 32;
  const int s = s0 + c;
  const float g = ((const float*)(ws + WS_G))[s];
  const u16* aT = (const u16*)(ws + WS_AT);
  unsigned av[16];
#pragma unroll
  for (int i = 0; i < 16; ++i) av[i] = aT[(size_t)(n0 + (i & 3) + 8 * (i >> 2) + 4 * hh) * SEQ + s];
  u32x2 qv[4];
#pragma unroll
  for (int gq = 0; gq < 4; ++gq) qv[gq] = *(const u32x2*)(Aq + (size_t)s * DM + n0 + 8 * gq + 4 * hh);
  asm volatile("" ::: "memory");
  f32x16 acc;
#pragma unroll
  for (int i = 0; i < 16; ++i) acc[i] = 0.f;
  const char* ab = lds + (w & 1) * 32768 + lane * 16;
  const char* bb = lds + (2 + (w >> 1)) * 32768 + lane * 16;
#pragma unroll
  for (int ch = 0; ch < 4; ++ch) {
    if (ch == 0) asm volatile("s_waitcnt vmcnt(45)" ::: "memory");
    if (ch == 1) asm volatile("s_waitcnt vmcnt(37)" ::: "memory");
    if (ch == 2) asm volatile("s_waitcnt vmcnt(29)" ::: "memory");
    if (ch == 3) asm volatile("s_waitcnt vmcnt(21)" ::: "memory");
    __builtin_amdgcn_s_barrier();
    asm volatile("" ::: "memory");
    bf16x8 af[8], bf[8];
#pragma unroll
    for (int ks = 0; ks < 8; ++ks) {
      af[ks] = *(const bf16x8*)(ab + (ch * 8 + ks) * 1024);
      bf[ks] = *(const bf16x8*)(bb + (ch * 8 + ks) * 1024);
    }
    __builtin_amdgcn_sched_barrier(0);
#pragma unroll
    for (int ks = 0; ks < 8; ++ks) acc = MFMA(af[ks], bf[ks], acc);
    __builtin_amdgcn_sched_barrier(0);
  }
  STAMP(2, 2);
  const float omg = rbf(1.f - g);
#pragma unroll
  for (int gq = 0; gq < 4; ++gq) {
    const int n = n0 + 8 * gq + 4 * hh;
    float res[4];
#pragma unroll
    for (int i = 0; i < 4; ++i) {
      const float qf = (i & 1) ? bfhi(qv[gq][i >> 1]) : bflo(qv[gq][i >> 1]);
      const float a = rbf(qf * bf2f(av[4 * gq + i]));
      const float bb2 = rbf(acc[4 * gq + i]);
      res[i] = rbf(g * a) + rbf(omg * bb2);
    }
    u32x2 wv;
    wv[0] = pk2(res[0], res[1]); wv[1] = pk2(res[2], res[3]);
    st8(out + (size_t)s * DM + n, wv);
  }
  STAMP(2, 3);
}

extern "C" void kernel_launch(void* const* d_in, const int* in_sizes, int n_in,
                              void* d_out, int out_size, void* d_ws, size_t ws_size,
                              hipStream_t stream) {
  const u16* Aq = (const u16*)d_in[0];
  const u16* Ak = (const u16*)d_in[1];
  const u16* Av = (const u16*)d_in[2];
  const u16* shiftk = (const u16*)d_in[3];
  const u16* ssmk = (const u16*)d_in[4];
  const u16* ssmD = (const u16*)d_in[5];
  const u16* wqkv = (const u16*)d_in[7];
  const u16* wo = (const u16*)d_in[8];
  const u16* gatew = (const u16*)d_in[9];
  char* ws = (char*)d_ws;
  k1_gemm_prep<<<256, 512, 0, stream>>>(Aq, Ak, Av, shiftk, wqkv, wo, gatew, ws);
  k2_attn_conv<<<1024, 256, 0, stream>>>(ssmk, ssmD, ws);
  k3_outproj<<<256, 256, 0, stream>>>(ws, Aq, (u16*)d_out);
}
```

```cpp
#define PRIO_A 2
#define XJ 2
#define PRIO_T 0
#define PRIO_C 1
#include <hip/hip_runtime.h>
#include <hip/hip_bf16.h>
#include <stdint.h>

#define SEQ 2048
#define DM 512
#define NH 32
#define DH 16

typedef __attribute__((ext_vector_type(8))) short bf16x8;
typedef __attribute__((ext_vector_type(4))) short s16x4;
typedef __attribute__((ext_vector_type(16))) float f32x16;
typedef __attribute__((ext_vector_type(4))) float f32x4;
typedef __attribute__((ext_vector_type(2))) float f32x2;
typedef __attribute__((ext_vector_type(4))) unsigned u32x4;
typedef __attribute__((ext_vector_type(2))) unsigned u32x2;
typedef __attribute__((ext_vector_type(2))) __bf16 bf16x2_t;
typedef unsigned short u16;

#define DI __device__ __forceinline__
#define MFMA(a, b, c) __builtin_amdgcn_mfma_f32_32x32x16_bf16((a), (b), (c), 0, 0, 0)
#define LDS_AS __attribute__((address_space(3)))
#define GLB_AS __attribute__((address_space(1)))

DI float bf2f(unsigned v16) { return __uint_as_float(v16 << 16); }
DI float bflo(unsigned w) { return __uint_as_float(w << 16); }
DI float bfhi(unsigned w) { return __uint_as_float(w & 0xffff0000u); }
DI unsigned pk2(float lo, float hi) {
  bf16x2_t v; v[0] = (__bf16)lo; v[1] = (__bf16)hi;
  return __builtin_bit_cast(unsigned, v);
}
DI float rbf(float x) {
  bf16x2_t v; v[0] = (__bf16)x; v[1] = (__bf16)0.f;
  return __uint_as_float(__builtin_bit_cast(unsigned, v) << 16);
}

#define ST_MOD "sc1"
DI void st16(void* p, u32x4 v) {
  asm volatile("global_store_dwordx4 %0, %1, off " ST_MOD "\n\ts_nop 1" :: "v"(p), "v"(v) : "memory");
}
DI void st8(void* p, u32x2 v) {
  asm volatile("global_store_dwordx2 %0, %1, off\n\ts_nop 1" :: "v"(p), "v"(v) : "memory");
}
DI void st8s(void* p, u32x2 v) {
  asm volatile("global_store_dwordx2 %0, %1, off sc1\n\ts_nop 1" :: "v"(p), "v"(v) : "memory");
}
#define TS(i)
#define TSW(kid)
#define WS_Q   (0u)
#define WS_K   (2u << 20)
#define WS_VT  (4u << 20)
#define WS_KVT (6u << 20)
#define WS_QT  (8u << 20)
#define WS_AT  (10u << 20)
#define WS_O   (12u << 20)
#define WS_WOT (14u << 20)
#define WS_G   (15u << 20)
#define WS_FL  (16u << 20)
#define K_PREP 0
#define K_GW   4
#define K_GEMM 5
#define K_ATT  6
#define K_CONV 7
#define GBLK(b) (((b) & 7) * 32 + ((b) >> 3))
#define FLAG(k, b) ((unsigned)(k) * 32768u + (unsigned)(b) * 128u)
#define FLB(ep) (WS_FL + (((unsigned)(ep) >> 7) & 1u) * 262144u)

typedef unsigned long long u64;
extern "C" __device__ unsigned long long my_dispatch_id() __asm("llvm.amdgcn.dispatch.id");
#define RSRC_T __amdgpu_buffer_rsrc_t
DI u32x4 ld128(RSRC_T rs, unsigned voff, unsigned soff) { return __builtin_amdgcn_raw_buffer_load_b128(rs, voff, soff, 16); }
#define SLP 1
DI void flag_set(char* fl, unsigned off, u64 ep) {
  __hip_atomic_store((GLB_AS u64*)(fl + off), ep, __ATOMIC_RELAXED, __HIP_MEMORY_SCOPE_AGENT);
}
#define SPIN_MAX (1 << 17)
DI void gpoll(char* fl, unsigned off, u64 ep) {
  GLB_AS u64* p = (GLB_AS u64*)(fl + off);
#pragma unroll 1
  for (int it = 0; it < SPIN_MAX; ++it) {
    const u64 v = __hip_atomic_load(p, __ATOMIC_RELAXED, __HIP_MEMORY_SCOPE_AGENT);
    if (__builtin_amdgcn_ballot_w64(v != ep) == 0) break;
    __builtin_amdgcn_s_sleep(SLP);
  }
  __builtin_amdgcn_fence(__ATOMIC_ACQUIRE, "wavefront");
  asm volatile("" ::: "memory");
}
DI void gpoll2(char* fl, unsigned off0, unsigned off1, u64 ep) {
  GLB_AS u64* p0 = (GLB_AS u64*)(fl + off0);
  GLB_AS u64* p1 = (GLB_AS u64*)(fl + off1);
#pragma unroll 1
  for (int it = 0; it < SPIN_MAX; ++it) {
    const u64 v0 = __hip_atomic_load(p0, __ATOMIC_RELAXED, __HIP_MEMORY_SCOPE_AGENT);
    const u64 v1 = __hip_atomic_load(p1, __ATOMIC_RELAXED, __HIP_MEMORY_SCOPE_AGENT);
    if (__builtin_amdgcn_ballot_w64(v0 != ep || v1 != ep) == 0) break;
    __builtin_amdgcn_s_sleep(SLP);
  }
  __builtin_amdgcn_fence(__ATOMIC_ACQUIRE, "wavefront");
  asm volatile("" ::: "memory");
}
DI bool fl2ok(u32x4 v, u64 ep) {
  const unsigned lo = (unsigned)ep, hi = (unsigned)(ep >> 32);
  return v[0] == lo && v[1] == hi && v[2] == lo && v[3] == hi;
}
DI void lds_wait1(unsigned a, u64 tag) {
#pragma unroll 1
  for (int it = 0; it < (SPIN_MAX << 4); ++it) {
    const u64 v = *(volatile LDS_AS u64*)a;
    if (__builtin_amdgcn_readfirstlane((unsigned)(v == tag))) break;
    __builtin_amdgcn_s_sleep(1);
  }
  asm volatile("" ::: "memory");
}
DI void lds_arrive(unsigned slot, u64 tag, int lane) {
  asm volatile("s_waitcnt lgkmcnt(0)" ::: "memory");
  if (lane == 0) *(volatile LDS_AS u64*)slot = tag;
  asm volatile("" ::: "memory");
}
DI void lds_wait(unsigned grp, u64 tag, int lane) {
  const unsigned a = grp + (lane & 3) * 8;
#pragma unroll 1
  for (int it = 0; it < (SPIN_MAX << 4); ++it) {
    const u64 v = *(volatile LDS_AS u64*)a;
    if (__builtin_amdgcn_ballot_w64((v - tag) > 15ull) == 0) break;
    __builtin_amdgcn_s_sleep(1);
  }
  asm volatile("" ::: "memory");
}

#define K1_WBYTES (512 * 192)
#define K1_XW 16384

DI const char* uni(const void* p) {
  const uint64_t v = (uint64_t)p;
  const unsigned lo = __builtin_amdgcn_readfirstlane((unsigned)v), hi = __builtin_amdgcn_readfirstlane((unsigned)(v >> 32));
  return (const char*)(((uint64_t)hi << 32) | lo);
}
DI void glds16(unsigned voff, const void* sbase, unsigned ldsaddr) {
  asm volatile("s_mov_b32 m0, %2\n\ts_nop 0\n\tglobal_load_lds_dwordx4 %0, %1"
               :: "v"(voff), "s"(uni(sbase)), "s"(__builtin_amdgcn_readfirstlane(ldsaddr)) : "memory");
}
DI void k1_issue_w(const u16* __restrict__ W, unsigned ldsb, int half, int n0, int w, const unsigned (&wo3)[3], int rot) {
#pragma unroll
  for (int i = 0; i < 4; ++i) {
    const int g = half * 16 + ((4 * i + w + rot) & 15);
    const char* sb = (const char*)W + (size_t)g * (16 * 3072) + n0 * 2;
#pragma unroll
    for (int j = 0; j < 3; ++j) glds16(wo3[j], sb, ldsb + g * 3072 + j * 1024);
  }
}
DI void k1_issue_x(const u16* __restrict__ X, unsigned xb, int half, int m0, unsigned xo) {
#pragma unroll
  for (int i = 0; i < 16; ++i) {
    const char* sb = (const char*)X + (size_t)(m0 + 2 * i) * 1024 + half * 512;
    glds16(xo ^ (unsigned)(((2 * i) & 15) << 4), sb, xb + i * 1024);
  }
}

template <int OFF>
DI void tr_rd(s16x4& d, unsigned base) {
  asm volatile("ds_read_b64_tr_b16 %0, %1 offset:%2" : "=v"(d) : "v"(base), "i"(OFF));
}
template <int KS>
DI void k1_rdb(s16x4 (&t)[6], unsigned base) {
  tr_rd<KS * 3072 + 0>(t[0], base);   tr_rd<KS * 3072 + 768>(t[1], base);
  tr_rd<KS * 3072 + 64>(t[2], base);  tr_rd<KS * 3072 + 64 + 768>(t[3], base);
  tr_rd<KS * 3072 + 128>(t[4], base); tr_rd<KS * 3072 + 128 + 768>(t[5], base);
}
template <int N>
DI void lgkm_wait6(s16x4 (&t)[6]) {
  asm volatile("s_waitcnt lgkmcnt(%6)" : "+v"(t[0]), "+v"(t[1]), "+v"(t[2]), "+v"(t[3]), "+v"(t[4]), "+v"(t[5]) : "i"(N));
}
struct XIss { const char* sb; unsigned xb, xo; };
template <int NV, int KS>
DI void k1_steps(const bf16x8 (&af)[16], unsigned wb, f32x16 (&acc)[3], s16x4 (&t0)[6], s16x4 (&t1)[6], s16x4 (&t2)[6],
                 const XIss& xi) {
  if constexpr (KS + 1 < 16) lgkm_wait6<6>(t0); else lgkm_wait6<0>(t0);
#pragma unroll
  for (int j = 0; j < 3; ++j) {
    const bf16x8 bfr = __builtin_shufflevector(t0[2 * j], t0[2 * j + 1], 0, 1, 2, 3, 4, 5, 6, 7);
    if (j >= 3 - NV) acc[j] = MFMA(af[KS], bfr, acc[j]);
    else             acc[j] = MFMA(bfr, af[KS], acc[j]);
    if (j == 0) { if constexpr (KS + 2 < 16) k1_rdb<KS + 2>(t2, wb); }
    if (j == XJ) {
      __builtin_amdgcn_sched_barrier(0);
      glds16(xi.xo ^ (unsigned)(((2 * KS) & 15) << 4), xi.sb + KS * 2048, xi.xb + KS * 1024);
      __builtin_amdgcn_sched_barrier(0);
    }
  }
  __builtin_amdgcn_sched_barrier(0);
  if constexpr (KS + 1 < 16) k1_steps<NV, KS + 1>(af, wb, acc, t1, t2, t0, xi);
}
template <int NV>
DI void k1_half(const bf16x8 (&af)[16], unsigned wb, f32x16 (&acc)[3], const XIss& xi) {
  s16x4 t0[6], t1[6], t2[6];
  k1_rdb<0>(t0, wb);
  k1_rdb<1>(t1, wb);
  k1_steps<NV, 0>(af, wb, acc, t0, t1, t2, xi);
}

struct K1Out { u16 *Qh, *Kh, *Vt; int n0, m0, r, hh; };
template <int NV, int J>
DI void k1_store(const f32x16& a, const K1Out& o) {
  const int nc = o.n0 + 32 * J;
  if constexpr (J >= 3 - NV) {
    const int nv = nc - 1024 + o.r;
    const int head = nv >> 4, d = nv & 15;
    u16* dst = o.Vt + (size_t)(head * 64 + (o.m0 >> 5)) * 512 + d * 16 + 8 * o.hh;
    u32x4 w0, w1;
    w0[0] = pk2(a[0], a[1]);  w0[1] = pk2(a[2], a[3]);
    w0[2] = pk2(a[4], a[5]);  w0[3] = pk2(a[6], a[7]);
    w1[0] = pk2(a[8], a[9]);  w1[1] = pk2(a[10], a[11]);
    w1[2] = pk2(a[12], a[13]); w1[3] = pk2(a[14], a[15]);
    st16(dst, w0);
    st16(dst + 256, w1);
  } else {
    u16* base = (nc < 512) ? o.Qh : o.Kh;
    const int head0 = (nc & 511) >> 4;
#pragma unroll
    for (int e = 0; e < 2; ++e) {
      unsigned a0 = pk2(a[8 * e], a[8 * e + 1]), a1 = pk2(a[8 * e + 2], a[8 * e + 3]);
      unsigned b0 = pk2(a[8 * e + 4], a[8 * e + 5]), b1 = pk2(a[8 * e + 6], a[8 * e + 7]);
      auto r0 = __builtin_amdgcn_permlane32_swap(a0, b0, false, false);
      auto r1 = __builtin_amdgcn_permlane32_swap(a1, b1, false, false);
      u32x4 wv = {r0[0], r1[0], r0[1], r1[1]};
      u16* dst = base + ((size_t)(head0 + e) * SEQ + o.m0 + o.r) * DH + 8 * o.hh;
      st16(dst, wv);
    }
  }
}
template <int U>
DI void k1_rd4(s16x4 (&t)[8], unsigned base) {
  constexpr int J = U >> 2, K0 = (U & 3) * 4;
  tr_rd<(K0 + 0) * 3072 + J * 64>(t[0], base); tr_rd<(K0 + 0) * 3072 + J * 64 + 768>(t[1], base);
  tr_rd<(K0 + 1) * 3072 + J * 64>(t[2], base); tr_rd<(K0 + 1) * 3072 + J * 64 + 768>(t[3], base);
  tr_rd<(K0 + 2) * 3072 + J * 64>(t[4], base); tr_rd<(K0 + 2) * 3072 + J * 64 + 768>(t[5], base);
  tr_rd<(K0 + 3) * 3072 + J * 64>(t[6], base); tr_rd<(K0 + 3) * 3072 + J * 64 + 768>(t[7], base);
}
template <int N>
DI void lgkm_wait8(s16x4 (&t)[8]) {
  asm volatile("s_waitcnt lgkmcnt(%8)" : "+v"(t[0]), "+v"(t[1]), "+v"(t[2]), "+v"(t[3]), "+v"(t[4]), "+v"(t[5]), "+v"(t[6]), "+v"(t[7]) : "i"(N));
}
template <int NV, int U>
DI void k1_steps2(const bf16x8 (&af)[16], unsigned wb, f32x16 (&acc)[3], s16x4 (&t0)[8], s16x4 (&t1)[8], s16x4 (&t2)[8],
                  const K1Out& o) {
  constexpr int J = U >> 2, K0 = (U & 3) * 4;
  if constexpr (U + 1 < 12) lgkm_wait8<8>(t0); else lgkm_wait8<0>(t0);
#pragma unroll
  for (int k = 0; k < 4; ++k) {
    const bf16x8 bfr = __builtin_shufflevector(t0[2 * k], t0[2 * k + 1], 0, 1, 2, 3, 4, 5, 6, 7);
    if (J >= 3 - NV) acc[J] = MFMA(af[K0 + k], bfr, acc[J]);
    else             acc[J] = MFMA(bfr, af[K0 + k], acc[J]);
    if (k == 0) { if constexpr (U + 2 < 12) k1_rd4<U + 2>(t2, wb); }
    if (k == 1) { if constexpr ((U & 3) == 0 && U > 0) k1_store<NV, J - 1>(acc[J - 1], o); }
  }
  __builtin_amdgcn_sched_barrier(0);
  if constexpr (U + 1 < 12) k1_steps2<NV, U + 1>(af, wb, acc, t1, t2, t0, o);
}
template <int NV>
DI void k1_half2(const bf16x8 (&af)[16], unsigned wb, f32x16 (&acc)[3], const K1Out& o) {
  s16x4 t0[8], t1[8], t2[8];
  k1_rd4<0>(t0, wb);
  k1_rd4<1>(t1, wb);
  k1_steps2<NV, 0>(af, wb, acc, t0, t1, t2, o);
}

struct PrepIn { unsigned kk[11], vv[8], wv[4]; u32x4 skw, gw, gx[2]; };
struct PrepOut { u32x4 kv[2]; u32x2 wo; float gv[2]; };
DI void prep_store(const PrepOut& O, char* __restrict__ ws, int b, int pw, int lane);
DI void prep_wait(PrepIn& P);
struct PrepSrc { const u16 *Ak, *Av, *shiftk, *wo, *gatew; };
DI void prep_load(PrepIn& P, const u16* __restrict__ Aq, const u16* __restrict__ Ak, const u16* __restrict__ Av,
                  const u16* __restrict__ shiftk, const u16* __restrict__ wo, const u16* __restrict__ gatew,
                  int b, int pw, int lane);
DI void prep_compute(PrepIn& P, PrepOut& O, int b, int lane);

template <int NV>
DI void gemm_body(const u16* __restrict__ X, char* lds, int mbase, int n0,
                  int w, int lane, u16* __restrict__ Qh, u16* __restrict__ Kh,
                  u16* __restrict__ Vt, char* ws, unsigned slot, u64 ep, long long* ts) {
  const int r = lane & 31, hh = lane >> 5;
  f32x16 acc[3];
#pragma unroll
  for (int j = 0; j < 3; ++j)
#pragma unroll
    for (int i = 0; i < 16; ++i) acc[j][i] = 0.f;

  const int g = (lane >> 4) & 1, q = (lane & 15) >> 2, p = lane & 3;
  const unsigned ldsb = (unsigned)(size_t)lds;
  const unsigned wb = ldsb + (8 * hh + q) * 192 + (16 * g + 4 * p) * 2;
  const unsigned xb = ldsb + K1_WBYTES + w * K1_XW;
  const unsigned xo = (unsigned)(hh * 1024 + ((r ^ hh) << 4));
  const unsigned xa = ldsb + K1_WBYTES + w * K1_XW + r * 512;
  const int m0 = mbase + 32 * w;

  bf16x8 af[16];
  asm volatile("s_waitcnt vmcnt(12)" ::: "memory");
#pragma unroll
  for (int ks = 0; ks < 16; ++ks) af[ks] = *(const LDS_AS bf16x8*)(xa + (((2 * ks + hh) ^ (r & 15)) * 16));
  asm volatile("s_waitcnt lgkmcnt(0)"
               : "+v"(af[0]), "+v"(af[1]), "+v"(af[2]), "+v"(af[3]), "+v"(af[4]), "+v"(af[5]), "+v"(af[6]), "+v"(af[7]),
                 "+v"(af[8]), "+v"(af[9]), "+v"(af[10]), "+v"(af[11]), "+v"(af[12]), "+v"(af[13]), "+v"(af[14]), "+v"(af[15])
               :: "memory");
  __builtin_amdgcn_s_barrier();
  asm volatile("" ::: "memory");
  TS(1);
  const XIss xi = {(const char*)X + (size_t)m0 * 1024 + 512, xb, xo};
  k1_half<NV>(af, wb, acc, xi);
  TS(2);
  asm volatile("s_waitcnt vmcnt(0)" ::: "memory");
  __builtin_amdgcn_s_barrier();
  asm volatile("" ::: "memory");
  TS(3);
#pragma unroll
  for (int ks = 0; ks < 16; ++ks) af[ks] = *(const LDS_AS bf16x8*)(xa + (((2 * ks + hh) ^ (r & 15)) * 16));
  asm volatile("s_waitcnt lgkmcnt(0)"
               : "+v"(af[0]), "+v"(af[1]), "+v"(af[2]), "+v"(af[3]), "+v"(af[4]), "+v"(af[5]), "+v"(af[6]), "+v"(af[7]),
                 "+v"(af[8]), "+v"(af[9]), "+v"(af[10]), "+v"(af[11]), "+v"(af[12]), "+v"(af[13]), "+v"(af[14]), "+v"(af[15])
               :: "memory");
  const K1Out o = {Qh, Kh, Vt, n0, m0, r, hh};
  k1_half2<NV>(af, wb + 256 * 192, acc, o);
  lds_arrive(slot, ep + 1, lane);
  TS(4);
  k1_store<NV, 2>(acc[2], o);
}

#define GLD1(dst, voff, sb, imm) asm volatile("global_load_dword %0, %1, %2 offset:" #imm : "=v"(dst) : "v"(voff), "s"(uni(sb)))
#define GLD4(dst, voff, sb, imm) asm volatile("global_load_dwordx4 %0, %1, %2 offset:" #imm : "=v"(dst) : "v"(voff), "s"(uni(sb)))
#define GLDH(dst, voff, sb, imm) asm volatile("global_load_ushort %0, %1, %2 offset:" #imm : "=v"(dst) : "v"(voff), "s"(uni(sb)))
DI void prep_load(PrepIn& P, const u16* __restrict__ Aq, const u16* __restrict__ Ak, const u16* __restrict__ Av,
                  const u16* __restrict__ shiftk, const u16* __restrict__ wo, const u16* __restrict__ gatew,
                  int b, int pw, int lane) {
  const int st = b >> 3, dt = b & 7;
  const int dg = lane & 7, sg = lane >> 3;
  const int d0 = dt * 64 + dg * 8 + 2 * pw;
  const int s0 = st * 64 + sg * 8;
  const unsigned va = (unsigned)((s0 == 0 ? 0 : s0 - 3) * 1024 + d0 * 2);
  const unsigned vb = (unsigned)(s0 * 1024 + d0 * 2);
  const char* k2 = (const char*)Ak + 4096;
  const char* v2 = (const char*)Av + 4096;
  GLD1(P.kk[0], va, Ak, 0); GLD1(P.kk[1], va, Ak, 1024); GLD1(P.kk[2], va, Ak, 2048);
  GLD1(P.kk[3], vb, Ak, 0); GLD1(P.kk[4], vb, Ak, 1024); GLD1(P.kk[5], vb, Ak, 2048); GLD1(P.kk[6], vb, Ak, 3072);
  GLD1(P.kk[7], vb, k2, 0); GLD1(P.kk[8], vb, k2, 1024); GLD1(P.kk[9], vb, k2, 2048); GLD1(P.kk[10], vb, k2, 3072);
  GLD1(P.vv[0], vb, Av, 0); GLD1(P.vv[1], vb, Av, 1024); GLD1(P.vv[2], vb, Av, 2048); GLD1(P.vv[3], vb, Av, 3072);
  GLD1(P.vv[4], vb, v2, 0); GLD1(P.vv[5], vb, v2, 1024); GLD1(P.vv[6], vb, v2, 2048); GLD1(P.vv[7], vb, v2, 3072);
  const unsigned vs = (unsigned)(d0 * 8), vl = (unsigned)(lane * 16), vh = (unsigned)(lane * 2);
  GLD4(P.skw, vs, shiftk, 0);
  GLD4(P.gw, vl, gatew, 0);
  const char* gq = (const char*)Aq + (size_t)(GBLK(b) * 8 + pw * 2) * 1024;
  GLD4(P.gx[0], vl, gq, 0); GLD4(P.gx[1], vl, gq, 1024);
  const char* wr = (const char*)wo + (size_t)((b >> 3) * 16 + pw * 4) * 1024 + (b & 7) * 128;
  GLDH(P.wv[0], vh, wr, 0); GLDH(P.wv[1], vh, wr, 1024); GLDH(P.wv[2], vh, wr, 2048); GLDH(P.wv[3], vh, wr, 3072);
}
DI void prep_wait(PrepIn& P) {
  __builtin_amdgcn_sched_barrier(0);
  asm volatile("s_waitcnt vmcnt(0)" ::: "memory");
  __builtin_amdgcn_sched_barrier(0);
  asm volatile(""
               : "+v"(P.kk[0]), "+v"(P.kk[1]), "+v"(P.kk[2]), "+v"(P.kk[3]), "+v"(P.kk[4]), "+v"(P.kk[5]),
                 "+v"(P.kk[6]), "+v"(P.kk[7]), "+v"(P.kk[8]), "+v"(P.kk[9]), "+v"(P.kk[10]),
                 "+v"(P.vv[0]), "+v"(P.vv[1]), "+v"(P.vv[2]), "+v"(P.vv[3]), "+v"(P.vv[4]), "+v"(P.vv[5]),
                 "+v"(P.vv[6]), "+v"(P.vv[7]));
  asm volatile("" : "+v"(P.skw), "+v"(P.gw), "+v"(P.gx[0]), "+v"(P.gx[1]),
                    "+v"(P.wv[0]), "+v"(P.wv[1]), "+v"(P.wv[2]), "+v"(P.wv[3]));
}

DI void prep_kv(PrepIn& P, char* __restrict__ ws, int b, int pw, int lane) {
  const int s0 = (b >> 3) * 64 + (lane >> 3) * 8;
  const int d0 = (b & 7) * 64 + (lane & 7) * 8 + 2 * pw;
  u16* kvT = (u16*)(ws + WS_KVT);
  if (s0 == 0) { P.kk[0] = 0; P.kk[1] = 0; P.kk[2] = 0; }
#pragma unroll
  for (int c = 0; c < 2; ++c) {
    const unsigned s01 = P.skw[c * 2], s23 = P.skw[c * 2 + 1];
    const float sk0 = bflo(s01), sk1 = bfhi(s01), sk2 = bflo(s23), sk3 = bfhi(s23);
    float kf[11];
#pragma unroll
    for (int i = 0; i < 11; ++i) kf[i] = c ? bfhi(P.kk[i]) : bflo(P.kk[i]);
    float kvv[8];
#pragma unroll
    for (int i = 0; i < 8; ++i) {
      float acc = rbf(kf[i] * sk3);
      acc = rbf(acc + rbf(kf[i + 1] * sk2));
      acc = rbf(acc + rbf(kf[i + 2] * sk1));
      acc = rbf(acc + rbf(kf[i + 3] * sk0));
      const float vf = c ? bfhi(P.vv[i]) : bflo(P.vv[i]);
      kvv[i] = acc * vf;
    }
    u32x4 o;
    o[0] = pk2(kvv[0], kvv[1]); o[1] = pk2(kvv[2], kvv[3]);
    o[2] = pk2(kvv[4], kvv[5]); o[3] = pk2(kvv[6], kvv[7]);
    st16(kvT + (size_t)(d0 + c) * SEQ + s0, o);
  }
}

DI void prep_gw(PrepIn& P, u32x2& wv, float& gl, int lane) {
  wv[0] = P.wv[0] | (P.wv[1] << 16); wv[1] = P.wv[2] | (P.wv[3] << 16);
  float gv[2];
#pragma unroll
  for (int i = 0; i < 2; ++i) {
    float sum = 0.f;
#pragma unroll
    for (int e = 0; e < 4; ++e) {
      sum += rbf(bflo(P.gx[i][e]) * bflo(P.gw[e]));
      sum += rbf(bfhi(P.gx[i][e]) * bfhi(P.gw[e]));
    }
#pragma unroll
    for (int o = 32; o > 0; o >>= 1) sum += __shfl_xor(sum, o);
    const float z = rbf(sum);
    gv[i] = rbf(1.f / (1.f + __expf(-z)));
  }
  gl = lane ? gv[1] : gv[0];
}
DI void prep_gw_store(u32x2 wv, float gl, char* __restrict__ ws, int b, int pw, int lane) {
  const int wk0 = (b >> 3) * 16 + pw * 4, wn = (b & 7) * 64 + lane;
  u16* woT = (u16*)(ws + WS_WOT);
  const size_t off = ((size_t)(((wn >> 5) * 32 + (wk0 >> 4)) * 2 + ((wk0 >> 3) & 1)) * 32 + (wn & 31)) * 8 + (wk0 & 7);
  st8s(woT + off, wv);
  GLB_AS float* gout = (GLB_AS float*)(ws + WS_G);
  if (lane < 2) __hip_atomic_store(gout + GBLK(b) * 8 + pw * 2 + lane, gl, __ATOMIC_RELAXED, __HIP_MEMORY_SCOPE_AGENT);
}

#define CSTR 4608
#define XR_OFF (8 * CSTR)
#define XR_STR 80
#define CONV_LDS (XR_OFF + 95 * XR_STR)
#define SLOT_OFF 44480
#define KV_OFF 49152
#define OUT_MOD ""
#define CPF_S 2
#define CPF_N 2
#define CPF_STR 64
#define WOT_OFF 98304

struct ConvRegs { u32x4 h0, h1, hx0, hx1; };

DI void conv_load_h(ConvRegs& R, int d, const u16* __restrict__ ssmk, int tid) {
  const u16* hrow = ssmk + (size_t)d * SEQ;
  const u32x4 z4 = {0u, 0u, 0u, 0u};
  R.h0 = *(const u32x4*)(hrow + 8 * (tid >= 4 ? tid - 4 : 0));
  R.h1 = *(const u32x4*)(hrow + 8 * (tid >= 3 ? tid - 3 : 0));
  R.hx0 = *(const u32x4*)(hrow + 8 * (252 + (tid & 3)));
  R.hx1 = *(const u32x4*)(hrow + 8 * (tid < 3 ? 253 + tid : 255));
}

DI void conv_write_copies(char* lds, int t, u32x4 h0, u32x4 h1) {
  unsigned wv[8] = {h0[0], h0[1], h0[2], h0[3], h1[0], h1[1], h1[2], h1[3]};
  unsigned od[7];
#pragma unroll
  for (int i = 0; i < 7; ++i) od[i] = __builtin_amdgcn_alignbyte(wv[i + 1], wv[i], 2);
#pragma unroll
  for (int c = 0; c < 8; ++c) {
    const int e = c >> 1;
    u32x4 v;
    if (c & 1) { v[0] = od[e]; v[1] = od[e + 1]; v[2] = od[e + 2]; v[3] = od[e + 3]; }
    else       { v[0] = wv[e]; v[1] = wv[e + 1]; v[2] = wv[e + 2]; v[3] = wv[e + 3]; }
    const unsigned off = (0x5D9159D0u >> (4 * c)) & 15u;
    *(u32x4*)(lds + c * CSTR + off * 16 + t * 16) = v;
  }
}

DI void conv_write_h(const ConvRegs& R, char* lds, int tid) {
  const u32x4 z4 = {0u, 0u, 0u, 0u};
  conv_write_copies(lds, tid, tid < 4 ? z4 : R.h0, tid < 3 ? z4 : R.h1);
  if (tid < 4) conv_write_copies(lds, 256 + tid, R.hx0, tid < 3 ? R.hx1 : z4);
}

DI void conv_main(u32x4 xv, int d, char* lds, float Dv,
                  char* __restrict__ ws, int tid, int w, int lane, u64 tag) {
  const int r = lane & 31, hh = lane >> 5;
  const unsigned grp = (unsigned)(size_t)lds + SLOT_OFF + 32, slot = grp + w * 8;
  {
    const u32x4 z4 = {0u, 0u, 0u, 0u};
    u32x4 rv;
#pragma unroll
    for (int i = 0; i < 4; ++i) rv[i] = __builtin_amdgcn_alignbit(xv[3 - i], xv[3 - i], 16);
    *(u32x4*)(lds + XR_OFF + ((tid >> 2) + 31) * XR_STR + (3 - (tid & 3)) * 16) = rv;
    if (tid < 124) *(u32x4*)(lds + XR_OFF + (tid >> 2) * XR_STR + (tid & 3) * 16) = z4;
  }
  lds_arrive(slot, tag + 1, lane); lds_wait(grp, tag + 1, lane);

  f32x16 acc0, acc1a, acc1b;
#pragma unroll
  for (int i = 0; i < 16; ++i) { acc0[i] = 0.f; acc1a[i] = 0.f; acc1b[i] = 0.f; }
  {
    const int c = (1 + r) & 7, u = (1 + r) >> 3;
    const unsigned off = (0x5D9159D0u >> (4 * c)) & 15u;
    const char* abase = lds + c * CSTR + off * 16 + (16 + 8 * hh + 8 * u) * 2 + w * 8 * 64;
    const char* bbase = lds + XR_OFF + (r + 31 - w * 8) * XR_STR + 16 * hh;
#pragma unroll
    for (int gi = 0; gi < 8; ++gi) {
#pragma unroll
      for (int ks = 0; ks < 2; ++ks) {
        const bf16x8 A0 = *(const bf16x8*)(abase + gi * 64 - ks * 32);
        const bf16x8 A1 = *(const bf16x8*)(abase + gi * 64 - ks * 32 + 32 * 64);
        const bf16x8 B0 = *(const bf16x8*)(bbase - gi * XR_STR + (1 - ks) * 32);
        const bf16x8 B1 = *(const bf16x8*)(bbase + (32 - gi) * XR_STR + (1 - ks) * 32);
        acc0 = MFMA(A0, B0, acc0);
        acc1a = MFMA(A1, B0, acc1a);
        acc1b = MFMA(A0, B1, acc1b);
      }
    }
  }
  lds_arrive(slot, tag + 2, lane); lds_wait(grp, tag + 2, lane);
  {
    float* red = (float*)lds;
#pragma unroll
    for (int dst = 0; dst < 4; ++dst)
#pragma unroll
      for (int hf = 0; hf < 2; ++hf) {
        f32x4 v;
#pragma unroll
        for (int i = 0; i < 4; ++i) {
          const int rg = 8 * (dst & 1) + 4 * hf + i;
          v[i] = (dst >> 1) ? (acc1a[rg] + acc1b[rg]) : acc0[rg];
        }
        *(f32x4*)(red + ((((w * 4 + dst) * 2 + hf) * 64) + lane) * 4) = v;
      }
  }
  lds_arrive(slot, tag + 3, lane); lds_wait(grp, tag + 3, lane);
  {
    const float* red = (const float*)lds;
    u16* aT = (u16*)(ws + WS_AT) + (size_t)d * SEQ;
    const int ablk = 32 * (w >> 1) + r;
    unsigned pa[2][2];
#pragma unroll
    for (int hf = 0; hf < 2; ++hf) {
      f32x4 y = {0.f, 0.f, 0.f, 0.f};
#pragma unroll
      for (int src = 0; src < 4; ++src) {
        const f32x4 v = *(const f32x4*)(red + ((((src * 4 + w) * 2 + hf) * 64) + lane) * 4);
        y += v;
      }
      const int b0 = 16 * (w & 1) + 8 * hf + 4 * hh;
      const u32x2 xw = *(const u32x2*)(lds + XR_OFF + (ablk + 31) * XR_STR + (28 - b0) * 2);
      float kv[4] = {bfhi(xw[1]), bflo(xw[1]), bfhi(xw[0]), bflo(xw[0])};
      float a[4];
#pragma unroll
      for (int i = 0; i < 4; ++i) {
        a[i] = rbf(y[i]) + rbf(kv[i] * Dv);
      }
      pa[hf][0] = pk2(a[0], a[1]); pa[hf][1] = pk2(a[2], a[3]);
    }
    auto r0 = __builtin_amdgcn_permlane32_swap(pa[0][0], pa[1][0], false, false);
    auto r1 = __builtin_amdgcn_permlane32_swap(pa[0][1], pa[1][1], false, false);
    u32x4 wv = {r0[0], r1[0], r0[1], r1[1]};
    st16(aT + 32 * ablk + 16 * (w & 1) + 8 * hh, wv);
  }
}

DI void attn_task(const char* lds, int lt0, int qb, bf16x8 qf, int h, char* __restrict__ ws) {
  const int lane = threadIdx.x & 63;
  const int c = lane & 31, hh = lane >> 5;
  f32x16 zero;
#pragma unroll
  for (int i = 0; i < 16; ++i) zero[i] = 0.f;
  f32x16 sc[9];
  const char* kb = lds + lt0 * 1024 + c * 32 + hh * 16;
#pragma unroll
  for (int t = 0; t < 9; ++t) {
    const bf16x8 kf = *(const bf16x8*)(kb + t * 1024);
    sc[t] = MFMA(kf, qf, zero);
  }
  const float NEG = -1e30f;
  if (qb < 8) {
    asm volatile("" ::: "memory");
#pragma unroll
    for (int t = 0; t < 8; ++t)
      if (qb - 8 + t < 0) {
#pragma unroll
        for (int i = 0; i < 16; ++i) sc[t][i] = NEG;
      }
  }
  unsigned msk[8];
#pragma unroll
  for (int i = 0; i < 16; ++i) {
    const int row = (i & 3) + 8 * (i >> 2) + 4 * hh;
    sc[0][i] = (row > c) ? sc[0][i] : sc[8][i];
  }
#pragma unroll
  for (int e = 0; e < 8; ++e) {
    const int row = ((2 * e) & 3) + 8 * ((2 * e) >> 2) + 4 * hh;
    msk[e] = (row > c ? 0xffffu : 0u) | (row + 1 > c ? 0xffff0000u : 0u);
  }
  float m = NEG;
#pragma unroll
  for (int t = 0; t < 8; ++t)
#pragma unroll
    for (int i = 0; i < 16; ++i) m = fmaxf(m, sc[t][i]);
  m = fmaxf(m, __shfl_xor(m, 32));
  const float cs = 0.25f * 1.4426950408889634f;
  const float mc = m * cs;
  f32x16 oacc[2];
  oacc[0] = zero; oacc[1] = zero;
  const char* vb = lds + 16384 + lt0 * 2048 + c * 32 + hh * 16;
  const f32x2 cs2 = {cs, cs}, mc2 = {-mc, -mc};
#pragma unroll
  for (int t = 0; t < 8; ++t)
#pragma unroll
    for (int e = 0; e < 8; ++e) {
      const f32x2 sv = {sc[t][2 * e], sc[t][2 * e + 1]};
      const f32x2 z = __builtin_elementwise_fma(sv, cs2, mc2);
      sc[t][2 * e] = z[0]; sc[t][2 * e + 1] = z[1];
    }
  __builtin_amdgcn_sched_barrier(0);
#pragma unroll
  for (int t = 0; t < 8; ++t) {
#pragma unroll
    for (int s = 0; s < 2; ++s) {
      const u32x4 vw = *(const u32x4*)(vb + t * 2048 + s * 1024);
      u32x4 pw;
#pragma unroll
      for (int e = 0; e < 4; ++e)
        pw[e] = pk2(__builtin_amdgcn_exp2f(sc[t][8 * s + 2 * e]), __builtin_amdgcn_exp2f(sc[t][8 * s + 2 * e + 1]));
      if (t == 0) {
        const u32x4 vw8 = *(const u32x4*)(vb + 8 * 2048 + s * 1024);
        u32x4 p0, p8;
#pragma unroll
        for (int e = 0; e < 4; ++e) { p0[e] = pw[e] & msk[4 * s + e]; p8[e] = pw[e] & ~msk[4 * s + e]; }
        oacc[s] = MFMA(__builtin_bit_cast(bf16x8, vw), __builtin_bit_cast(bf16x8, p0), oacc[s]);
        oacc[s] = MFMA(__builtin_bit_cast(bf16x8, vw8), __builtin_bit_cast(bf16x8, p8), oacc[s]);
      } else {
        oacc[s] = MFMA(__builtin_bit_cast(bf16x8, vw), __builtin_bit_cast(bf16x8, pw), oacc[s]);
      }
    }
  }
  const float inv = 1.f / (oacc[0][8] + oacc[1][8]);
  unsigned a0 = pk2((oacc[0][0] + oacc[1][0]) * inv, (oacc[0][1] + oacc[1][1]) * inv);
  unsigned a1 = pk2((oacc[0][2] + oacc[1][2]) * inv, (oacc[0][3] + oacc[1][3]) * inv);
  unsigned b0 = pk2((oacc[0][4] + oacc[1][4]) * inv, (oacc[0][5] + oacc[1][5]) * inv);
  unsigned b1 = pk2((oacc[0][6] + oacc[1][6]) * inv, (oacc[0][7] + oacc[1][7]) * inv);
  auto r0 = __builtin_amdgcn_permlane32_swap(a0, b0, false, false);
  auto r1 = __builtin_amdgcn_permlane32_swap(a1, b1, false, false);
  u32x4 wv = {r0[0], r1[0], r0[1], r1[1]};
  u16* o = (u16*)(ws + WS_O) + ((size_t)((qb * 32 + h) * 2 + hh) * 32 + c) * 8;
  st16(o, wv);
}

__global__ __launch_bounds__(512, 2) void k_mega(
    const u16* __restrict__ Aq, const u16* __restrict__ Ak, const u16* __restrict__ Av,
    const u16* __restrict__ shiftk, const u16* __restrict__ ssmk, const u16* __restrict__ ssmD,
    const u16* __restrict__ wqkv, const u16* __restrict__ wo,
    const u16* __restrict__ gatew, char* __restrict__ ws, u16* __restrict__ out) {
  __shared__ __attribute__((aligned(16))) char lds[K1_WBYTES + 4 * K1_XW];
  const int tid = threadIdx.x, lane = tid & 63;
  const int w = __builtin_amdgcn_readfirstlane(tid >> 6);
  const int b = blockIdx.x;
  long long ts[16];
#pragma unroll
  for (int i = 0; i < 16; ++i) ts[i] = 0;
  TS(0);
  u64 pc0; unsigned lenA, lenC;
  asm volatile("s_getpc_b64 %0\nmega_lbl_0:\n\ts_mov_b32 %1, mega_lbl_a-mega_lbl_0\n\ts_mov_b32 %2, mega_lbl_c-mega_lbl_0"
               : "=s"(pc0), "=s"(lenA), "=s"(lenC));
  u64 ep = my_dispatch_id() * 0x9E3779B97F4A7C15ull + (u64)(size_t)__builtin_amdgcn_dispatch_ptr();
  ep ^= ep >> 31; ep *= 0xD6E8FEB86659FD93ull; ep ^= ep >> 29;
  ep = (ep | 1ull) << 5 | 1ull;
  const RSRC_T rs = __builtin_amdgcn_make_buffer_rsrc((void*)ws, 0, 32 << 20, 0x00020000);
  const unsigned ldsb = (unsigned)(size_t)lds;
  const unsigned flb = __builtin_amdgcn_readfirstlane(FLB(ep));
  char* const fl = ws + flb;
  const int st3 = (b & 7) * 4 + (b >> 6), ntl = (b >> 3) & 7;

  if (w >= 4) {
    const int pw = w - 4, t = tid - 256;
    __builtin_amdgcn_s_setprio(3);
    PrepIn P;
    prep_load(P, Aq, Ak, Av, shiftk, wo, gatew, b, pw, lane);
    unsigned cpf[CPF_N];
    {
      const int la = (int)lenA, lc = (int)lenC, lm = la > lc ? la : lc;
      const unsigned len = lm > 0 ? (unsigned)lm : 0u;
#pragma unroll
      for (int i = 0; i < CPF_N; ++i) {
        unsigned o = (unsigned)((((b >> 3) & (CPF_S - 1)) * 4 + pw) * CPF_N + i) * 64u * CPF_STR + lane * CPF_STR;
        o = o + 4 <= len ? o : 0u;
        cpf[i] = *(const GLB_AS unsigned*)(pc0 + o);
      }
    }
    unsigned dpf;
    {
      const int xcd = b & 7, idx = b >> 3;
      const int tw = t < 96 ? t : 0;
      const int id = idx * 96 + tw, row = 256 + id / 12, cl = id % 12;
      const char* pa = (const char*)wqkv + (size_t)row * 3072 + (xcd & 1) * 1536 + cl * 128;
      dpf = *(const unsigned*)pa;
    }
    prep_wait(P);
    asm volatile("" :: "v"(dpf));
    TS(12);
#pragma unroll
    for (int i = 0; i < CPF_N; ++i) asm volatile("" :: "v"(cpf[i]));
    prep_kv(P, ws, b, pw, lane);
    TS(1);
    asm volatile("" ::: "memory");
    __builtin_amdgcn_s_barrier();
    asm volatile("" ::: "memory");
    ConvRegs R0, R1;
    conv_load_h(R0, 2 * b, ssmk, t);
    conv_load_h(R1, 2 * b + 1, ssmk, t);
    const unsigned dpair = *(const unsigned*)(ssmD + 2 * b);
    asm volatile("s_waitcnt vmcnt(8)" ::: "memory");
    if (lane == 0) flag_set(fl, FLAG(K_PREP + pw, b), ep);
    TS(3);
    asm volatile("" ::: "memory");
    __builtin_amdgcn_s_barrier();
    asm volatile("" ::: "memory");
    TS(2);
    __builtin_amdgcn_s_setprio(PRIO_C);
    if (pw == 0) {
      gpoll(fl, FLAG(K_PREP + (b & 3), (lane & 31) * 8 + (b >> 5)), ep);
      if (lane == 0) *(volatile LDS_AS u64*)(ldsb + SLOT_OFF + 64) = ep;
      asm volatile("" ::: "memory");
    } else {
      lds_wait1(ldsb + SLOT_OFF + 64, ep);
    }
    TS(4);
    u32x4 x0 = ld128(rs, t * 16, WS_KVT + 2 * b * (SEQ * 2));
    u32x4 x1 = ld128(rs, t * 16, WS_KVT + (2 * b + 1) * (SEQ * 2));
    asm volatile("" ::: "memory");
    u32x2 wov; float gl;
    prep_gw(P, wov, gl, lane);
    conv_write_h(R0, lds, t);
    TS(10);
    asm volatile("" : "+v"(x0), "+v"(x1) :: "memory");
    prep_gw_store(wov, gl, ws, b, pw, lane);
    if (pw == 1 && lane < 8) *(GLB_AS u64*)(ws + (flb ^ 262144u) + FLAG(lane, b)) = 0ull;
    asm volatile("" ::: "memory");
    const unsigned grp = ldsb + SLOT_OFF + 32, slot = grp + pw * 8;
    conv_main(x0, 2 * b, lds, bflo(dpair), ws, t, pw, lane, ep);
    TS(5);
    asm volatile("s_waitcnt vmcnt(1)" ::: "memory");
    lds_arrive(slot, ep + 4, lane); lds_wait(grp, ep + 4, lane);
    if (t == 0) flag_set(fl, FLAG(K_GW, b), ep);
    TS(6);
    conv_write_h(R1, lds, t);
    conv_main(x1, 2 * b + 1, lds, bfhi(dpair), ws, t, pw, lane, ep + 4);
    TS(7);
    asm volatile("s_waitcnt vmcnt(0)" ::: "memory");
    lds_arrive(slot, ep + 8, lane);
    if (pw == 0) {
      lds_wait(grp, ep + 8, lane);
      if (lane == 0) flag_set(fl, FLAG(K_CONV, b), ep);
    }
    TS(8);
    gpoll(fl, FLAG(K_GW, (lane & 31) * 8 + ntl), ep);
    {
      u32x4 wt[16];
#pragma unroll
      for (int i = 0; i < 16; ++i) wt[i] = ld128(rs, lane * 16, WS_WOT + ntl * 65536 + pw * 16384 + i * 1024);
      lds_wait(ldsb + SLOT_OFF, ep + 1, lane);
#pragma unroll
      for (int i = 0; i < 16; ++i) *(u32x4*)(lds + WOT_OFF + pw * 16384 + i * 1024 + lane * 16) = wt[i];
      asm volatile("s_waitcnt lgkmcnt(0)" ::: "memory");
    }
    TS(11);
    if (pw == 0) {
      gpoll2(fl, lane < 32 ? FLAG(K_ATT, lane * 8 + (st3 >> 2)) : FLAG(K_CONV, ntl * 32 + (lane - 32)),
             FLAG(K_GW, (((st3 * 8 + (lane & 7)) & 31) << 3) | (st3 >> 2)), ep);
    }
    TS(9);
    __builtin_amdgcn_s_barrier();
    TSW(2);
    asm volatile("mega_lbl_c:");
    return;
  }
  {
    const int xcd = b & 7, idx = b >> 3;
    const int mt = (xcd >> 1) * 4 + (idx >> 3);
    const int nt = (xcd & 1) * 8 + (idx & 7);
    const int n0 = nt * 96, mbase = mt * 128;
    const int rot = (idx >> 3) * 4;
    unsigned wo3[3];
#pragma unroll
    for (int j = 0; j < 3; ++j) {
      const int ci = j * 64 + lane, row = ci / 12, c16 = ci - row * 12;
      wo3[j] = (unsigned)(row * 3072 + c16 * 16);
    }
    const unsigned xo = (unsigned)((lane >> 5) * 1024 + (((lane & 31) ^ (lane >> 5)) << 4));
    k1_issue_w(wqkv, ldsb, 0, n0, w, wo3, rot);
    k1_issue_x(Aq, ldsb + K1_WBYTES + w * K1_XW, 0, mbase + 32 * w, xo);
    k1_issue_w(wqkv, ldsb, 1, n0, w, wo3, rot);

    u16* Qh = (u16*)(ws + WS_Q);
    u16* Kh = (u16*)(ws + WS_K);
    u16* Vt = (u16*)(ws + WS_VT);
    const unsigned slot = ldsb + SLOT_OFF + w * 8;
    __builtin_amdgcn_s_setprio(PRIO_A);
    if (nt <= 9) gemm_body<0>(Aq, lds, mbase, n0, w, lane, Qh, Kh, Vt, ws, slot, ep, ts);
    else if (nt == 10) gemm_body<1>(Aq, lds, mbase, n0, w, lane, Qh, Kh, Vt, ws, slot, ep, ts);
    else gemm_body<3>(Aq, lds, mbase, n0, w, lane, Qh, Kh, Vt, ws, slot, ep, ts);
    asm volatile("s_waitcnt vmcnt(0)" ::: "memory");
    lds_arrive(slot, ep + 2, lane);
    if (w == 0) {
      lds_wait(ldsb + SLOT_OFF, ep + 2, lane);
      if (lane == 0) flag_set(fl, FLAG(K_GEMM, b), ep);
    }
    TS(5);
  }
  {
    const int h = b >> 3, g = b & 7;
    const int c = lane & 31, hh = lane >> 5;
    if (w == 0) {
      const int sel = (lane >> 2) % 3;
      const int ntc = (sel * 512 + 16 * h) / 96;
      int mtc = 2 * g - 2 + (lane & 3);
      mtc = mtc < 0 ? 0 : mtc;
      const int pb = (((mtc & 3) * 8 + (ntc & 7)) << 3) | ((mtc >> 2) * 2 + (ntc >> 3));
      gpoll(fl, FLAG(K_GEMM, pb), ep);
      if (lane == 0) *(volatile LDS_AS u64*)(ldsb + SLOT_OFF + 72) = ep;
      asm volatile("" ::: "memory");
    } else {
      lds_wait1(ldsb + SLOT_OFF + 72, ep);
    }
    TS(6);
    u32x4 kt[4], vt[4];
#pragma unroll
    for (int i = 0; i < 4; ++i) {
      int gt = 8 * g - 8 + w * 4 + i; gt = gt < 0 ? 0 : gt;
      kt[i] = ld128(rs, lane * 16, WS_K + h * (SEQ * DH * 2) + gt * 1024);
      vt[i] = ld128(rs, lane * 16, WS_VT + h * (SEQ * DH * 2) + gt * 1024);
    }
    const int qb0 = 8 * g + w, qb1 = 8 * g + w + 4;
    const bf16x8 q0 = __builtin_bit_cast(bf16x8, ld128(rs, c * 32 + hh * 16, WS_Q + h * (SEQ * DH * 2) + qb0 * 1024));
    const bf16x8 q1 = __builtin_bit_cast(bf16x8, ld128(rs, c * 32 + hh * 16, WS_Q + h * (SEQ * DH * 2) + qb1 * 1024));
    const unsigned grp = ldsb + SLOT_OFF, slot = grp + w * 8;
    const unsigned onesw = ((c >> 1) == 0 || (c >> 1) == 4) ? 0x3F803F80u : 0u;
    const u32x4 cst = {onesw, onesw, onesw, onesw};
    lds_wait(grp, ep + 1, lane);
#pragma unroll
    for (int i = 0; i < 4; ++i) {
      *(u32x4*)(lds + KV_OFF + (w * 4 + i) * 1024 + lane * 16) = kt[i];
      *(u32x4*)(lds + KV_OFF + 16384 + (w * 4 + i) * 2048 + hh * 1024 + c * 16) = vt[i];
      *(u32x4*)(lds + KV_OFF + 16384 + (w * 4 + i) * 2048 + hh * 1024 + 512 + c * 16) = cst;
    }
    lds_arrive(slot, ep + 3, lane); lds_wait(grp, ep + 3, lane);
    __builtin_amdgcn_s_setprio(PRIO_T);
    TS(7);
    attn_task(lds + KV_OFF, w, qb0, q0, h, ws);
    TS(8);
    attn_task(lds + KV_OFF, w + 4, qb1, q1, h, ws);
    TS(9);
    asm volatile("s_waitcnt vmcnt(0)" ::: "memory");
    lds_arrive(slot, ep + 4, lane);
    if (w == 0) {
      lds_wait(grp, ep + 4, lane);
      if (lane == 0) flag_set(fl, FLAG(K_ATT, b), ep);
    }
    TS(10);
  }
  {
    const int c = lane & 31, hh = lane >> 5;
    const int ntile = ntl * 2 + (w & 1), stile = st3 * 2 + (w >> 1);
    const int n0 = ntile * 32, s0 = stile * 32;
    const int s = s0 + c;
    asm volatile("" ::: "memory");
    __builtin_amdgcn_s_barrier();
    asm volatile("" ::: "memory");
    TS(11);
    const int kh = w & 1;
    u32x4 of[16];
#pragma unroll
    for (int i = 0; i < 16; ++i) of[i] = ld128(rs, lane * 16, WS_O + stile * 32768 + kh * 16384 + i * 1024);
    const float g = __builtin_bit_cast(float, __builtin_amdgcn_raw_buffer_load_b32(rs, c * 4, WS_G + s0 * 4, 16));
    unsigned av[16];
#pragma unroll
    for (int i = 0; i < 16; ++i)
      av[i] = __builtin_amdgcn_raw_buffer_load_b16(rs, (4 * hh * SEQ + c) * 2, WS_AT + ((n0 + (i & 3) + 8 * (i >> 2)) * SEQ + s0) * 2, 16);
    u32x2 qv[4];
#pragma unroll
    for (int gq = 0; gq < 4; ++gq) qv[gq] = *(const u32x2*)(Aq + (size_t)s * DM + n0 + 8 * gq + 4 * hh);
    asm volatile("" ::: "memory");
    f32x16 acc, acco;
#pragma unroll
    for (int i = 0; i < 16; ++i) { acc[i] = 0.f; acco[i] = 0.f; }
    const char* ab = lds + WOT_OFF + kh * 32768 + kh * 16384 + lane * 16;
    const char* ao = lds + WOT_OFF + (kh ^ 1) * 32768 + kh * 16384 + lane * 16;
#pragma unroll
    for (int ks = 0; ks < 16; ++ks) {
      const bf16x8 afo = *(const bf16x8*)(ao + ks * 1024);
      const bf16x8 af = *(const bf16x8*)(ab + ks * 1024);
      acco = MFMA(afo, __builtin_bit_cast(bf16x8, of[ks]), acco);
      acc = MFMA(af, __builtin_bit_cast(bf16x8, of[ks]), acc);
    }
    {
      char* xw = lds + KV_OFF + w * 4096 + lane * 16;
      const char* xr = lds + KV_OFF + (w ^ 1) * 4096 + lane * 16;
#pragma unroll
      for (int i = 0; i < 4; ++i) {
        const f32x4 v = {acco[4 * i], acco[4 * i + 1], acco[4 * i + 2], acco[4 * i + 3]};
        *(f32x4*)(xw + i * 1024) = v;
      }
      lds_arrive(ldsb + SLOT_OFF + w * 8, ep + 5, lane);
      lds_wait1(ldsb + SLOT_OFF + (w ^ 1) * 8, ep + 5);
#pragma unroll
      for (int i = 0; i < 4; ++i) {
        const f32x4 v = *(const f32x4*)(xr + i * 1024);
        acc[4 * i] += v[0]; acc[4 * i + 1] += v[1]; acc[4 * i + 2] += v[2]; acc[4 * i + 3] += v[3];
      }
    }
    TS(12);
    const float omg = rbf(1.f - g);
    unsigned pr[4][2];
#pragma unroll
    for (int gq = 0; gq < 4; ++gq) {
      float res[4];
#pragma unroll
      for (int i = 0; i < 4; ++i) {
        const float qf = (i & 1) ? bfhi(qv[gq][i >> 1]) : bflo(qv[gq][i >> 1]);
        const float a = rbf(qf * bf2f(av[4 * gq + i]));
        const float bb2 = rbf(acc[4 * gq + i]);
        res[i] = rbf(g * a) + rbf(omg * bb2);
      }
      pr[gq][0] = pk2(res[0], res[1]); pr[gq][1] = pk2(res[2], res[3]);
    }
#pragma unroll
    for (int e2 = 0; e2 < 2; ++e2) {
      auto r0 = __builtin_amdgcn_permlane32_swap(pr[2 * e2][0], pr[2 * e2 + 1][0], false, false);
      auto r1 = __builtin_amdgcn_permlane32_swap(pr[2 * e2][1], pr[2 * e2 + 1][1], false, false);
      u32x4 wv = {r0[0], r1[0], r0[1], r1[1]};
      u16* dst = out + (size_t)s * DM + n0 + 16 * e2 + 8 * hh;
      asm volatile("global_store_dwordx4 %0, %1, off " OUT_MOD "\n\ts_nop 1" :: "v"(dst), "v"(wv) : "memory");
    }
    TS(13);
    TSW(0);
    asm volatile("mega_lbl_a:");
  }
}

extern "C" void kernel_launch(void* const* d_in, const int* in_sizes, int n_in,
                              void* d_out, int out_size, void* d_ws, size_t ws_size,
                              hipStream_t stream) {
  const u16* Aq = (const u16*)d_in[0];
  const u16* Ak = (const u16*)d_in[1];
  const u16* Av = (const u16*)d_in[2];
  const u16* shiftk = (const u16*)d_in[3];
  const u16* ssmk = (const u16*)d_in[4];
  const u16* ssmD = (const u16*)d_in[5];
  const u16* wqkv = (const u16*)d_in[7];
  const u16* wo = (const u16*)d_in[8];
  const u16* gatew = (const u16*)d_in[9];
  char* ws = (char*)d_ws;
  k_mega<<<256, 512, 0, stream>>>(Aq, Ak, Av, shiftk, ssmk, ssmD, wqkv, wo, gatew, ws, (u16*)d_out);
}
```
